```python
import torch

def _softmax(x, axis=-1):
    return torch.softmax(x, dim=axis)
def _gelu(x):
    return torch.nn.functional.gelu(x, approximate="tanh")
def _silu(x):
    return torch.nn.functional.silu(x)
def _sigmoid(x):
    return torch.sigmoid(x)
def _layernorm(x, axis=-1, eps=1e-5):
    m = x.mean(dim=axis, keepdim=True)
    v = x.var(dim=axis, keepdim=True, unbiased=False)
    return (x - m) / torch.sqrt(v + eps)
def _scatter_add(tgt, idx, src, axis=0):
    return tgt.clone().index_add_(axis, idx, src)
def _segment_sum(src, seg, n):
    out = torch.zeros((n,) + src.shape[1:], dtype=src.dtype)
    return out.index_add_(0, seg, src)
def _pad(x, pw):
    flat = tuple(v for pair in reversed(pw) for v in pair)
    return torch.nn.functional.pad(x, flat)

kernel_name = 'sequential_preLN__sliding_local_dilated__glu_gated_reglu__prompt'

def setup_inputs():

    _w = (lambda a: (lambda t: t.to(torch.bfloat16) if t.is_floating_point() else t)(torch.as_tensor(a)))
    def _A_setup():
        B, S, D = 1, 2048, 512
        return {
            "x": _w((lambda *s: torch.randn(*s, dtype=torch.bfloat16) * 0.05)(B, S, D)),
            "wqkv": _w((lambda *s: torch.randn(*s, dtype=torch.bfloat16) * 0.05)(D, 3 * D)),
            "wo": _w((lambda *s: torch.randn(*s, dtype=torch.bfloat16) * 0.05)(D, D)),
        }
    _A_in = _A_setup()

    def _B_setup():
        B, S, D, F = 1, 2048, 512, 1376
        return {
            "x": _w((lambda *s: torch.randn(*s, dtype=torch.bfloat16) * 0.05)(B, S, D)),
            "W_gate": _w((lambda *s: torch.randn(*s, dtype=torch.bfloat16) * 0.8)(D, F) * (2.0 / D) ** 0.5),
            "W_up": _w((lambda *s: torch.randn(*s, dtype=torch.bfloat16) * 0.8)(D, F) * (2.0 / D) ** 0.5),
            "W_down": _w((lambda *s: torch.randn(*s, dtype=torch.bfloat16) * 0.8)(F, D) * (1.0 / F) ** 0.5),
        }
    _B_in = _B_setup()

    return {
        "A_x": _A_in["x"],
        "A_wqkv": _A_in["wqkv"],
        "A_wo": _A_in["wo"],
        "B_x": _B_in["x"],
        "B_W_gate": _B_in["W_gate"],
        "B_W_up": _B_in["W_up"],
        "B_W_down": _B_in["W_down"],
    }

def reference(A_x, A_wqkv, A_wo, B_x, B_W_gate, B_W_up, B_W_down):
    def _A_ref(x, wqkv, wo):
        B, S, D = x.shape
        H = 32; Dh = D // H
        W, dil = 8, 2
        qkv = (lambda x, *d: x.permute(*d))((x @ wqkv).reshape(B, S, 3, H, Dh), 2, 0, 3, 1, 4)
        q, k, v = qkv[0], qkv[1], qkv[2]
        scale = Dh ** -0.5
        pos = torch.arange(S)
        off = torch.arange(W) * dil
        idx = pos[:, None] - off[None, :]
        mask = idx >= 0
        idx_c = torch.where(mask, idx, 0).reshape(-1)
        kg = k[:, :, idx_c, :].reshape(B, H, S, W, Dh)
        vg = v[:, :, idx_c, :].reshape(B, H, S, W, Dh)
        logits = torch.einsum("bhsd,bhswd->bhsw", q, kg) * scale
        logits = torch.where(mask[None, None, :, :], logits, -1e9)
        attn = _softmax(logits, axis=-1)
        o = torch.einsum("bhsw,bhswd->bhsd", attn, vg)
        return (lambda x, *d: x.permute(*d))(o, 0, 2, 1, 3).reshape(B, S, D) @ wo

    def _B_ref(x, W_gate, W_up, W_down):
        gate = torch.einsum("bsd,df->bsf", x, W_gate)
        up = torch.einsum("bsd,df->bsf", x, W_up)
        gate = gate * (lambda x, dt: x.to(dt))(gate > 0, gate.dtype)
        h = gate * up
        return torch.einsum("bsf,fd->bsd", h, W_down)

    x = A_x
    h = x + _A_ref(_layernorm(x, axis=-1), A_wqkv, A_wo)
    h2 = h + _B_ref(_layernorm(h, axis=-1), B_W_gate, B_W_up, B_W_down)
    return h2
```

```cpp
#include <hip/hip_runtime.h>
#include <hip/hip_bf16.h>
#include <stdint.h>

typedef unsigned short u16;
using bf16x8 = __attribute__((ext_vector_type(8))) short;
using f32x4 = __attribute__((ext_vector_type(4))) float;

__device__ __forceinline__ float bf2f(u16 b) { return __uint_as_float(((unsigned)b) << 16); }
__device__ __forceinline__ u16 f2bf(float f) {
  unsigned u = __float_as_uint(f);
  u += 0x7FFFu + ((u >> 16) & 1u);
  return (u16)(u >> 16);
}

constexpr int S = 2048, D = 512, F = 1376;

__global__ void ln_kernel(const u16* __restrict__ x, u16* __restrict__ y) {
  int row = blockIdx.x * 4 + (threadIdx.x >> 6);
  int lane = threadIdx.x & 63;
  const u16* xr = x + (size_t)row * D;
  float v[8];
  float s = 0.f;
  for (int j = 0; j < 8; ++j) { v[j] = bf2f(xr[lane * 8 + j]); s += v[j]; }
  for (int m = 32; m >= 1; m >>= 1) s += __shfl_xor(s, m);
  float mean = s * (1.0f / D);
  float q = 0.f;
  for (int j = 0; j < 8; ++j) { float d = v[j] - mean; q += d * d; }
  for (int m = 32; m >= 1; m >>= 1) q += __shfl_xor(q, m);
  float var = q * (1.0f / D);
  float rstd = rsqrtf(var + 1e-5f);
  for (int j = 0; j < 8; ++j) y[(size_t)row * D + lane * 8 + j] = f2bf((v[j] - mean) * rstd);
}

template <int MODE>
__global__ void gemm_kernel(const u16* __restrict__ A, const u16* __restrict__ B, const u16* __restrict__ R,
                            u16* __restrict__ C, int M, int N, int K) {
  __shared__ u16 sA[64][40];
  __shared__ u16 sB[64][40];
  int bm = blockIdx.y * 64, bn = blockIdx.x * 64;
  int tid = threadIdx.x, lane = tid & 63, w = tid >> 6;
  int wm = (w >> 1) * 32, wn = (w & 1) * 32;
  f32x4 acc[2][2] = {};
  for (int k0 = 0; k0 < K; k0 += 32) {
    {
      int r = tid >> 2, c = (tid & 3) * 8;
      bf16x8 v = *(const bf16x8*)(A + (size_t)(bm + r) * K + k0 + c);
      *(bf16x8*)&sA[r][c] = v;
    }
    {
      int k = tid >> 3, n = (tid & 7) * 8;
      bf16x8 v = {};
      if (bn + n < N) v = *(const bf16x8*)(B + (size_t)(k0 + k) * N + bn + n);
      for (int j = 0; j < 8; ++j) sB[n + j][k] = (u16)v[j];
    }
    __syncthreads();
    int fr = lane & 15, fq = lane >> 4;
    bf16x8 a[2], b[2];
    for (int i = 0; i < 2; ++i) {
      a[i] = *(const bf16x8*)&sA[wm + i * 16 + fr][fq * 8];
      b[i] = *(const bf16x8*)&sB[wn + i * 16 + fr][fq * 8];
    }
    for (int i = 0; i < 2; ++i)
      for (int j = 0; j < 2; ++j)
        acc[i][j] = __builtin_amdgcn_mfma_f32_16x16x32_bf16(a[i], b[j], acc[i][j], 0, 0, 0);
    __syncthreads();
  }
  int fr = lane & 15, fq = lane >> 4;
  for (int i = 0; i < 2; ++i)
    for (int j = 0; j < 2; ++j)
      for (int r = 0; r < 4; ++r) {
        int row = bm + wm + i * 16 + fq * 4 + r, col = bn + wn + j * 16 + fr;
        if (col < N) {
          float v = acc[i][j][r];
          if (MODE == 1) v = bf2f(R[(size_t)row * N + col]) + bf2f(f2bf(v));
          C[(size_t)row * N + col] = f2bf(v);
        }
      }
}

__global__ void attn_kernel(const u16* __restrict__ qkv, u16* __restrict__ o) {
  int idx = blockIdx.x * blockDim.x + threadIdx.x;
  int h = idx & 31, s = idx >> 5;
  const u16* q = qkv + (size_t)s * 1536 + h * 16;
  float qf[16];
  for (int d = 0; d < 16; ++d) qf[d] = bf2f(q[d]);
  float lg[8];
  float mx = -1e30f;
  for (int w = 0; w < 8; ++w) {
    int t = s - 2 * w;
    if (t >= 0) {
      const u16* k = qkv + (size_t)t * 1536 + 512 + h * 16;
      float a = 0.f;
      for (int d = 0; d < 16; ++d) a += qf[d] * bf2f(k[d]);
      lg[w] = a * 0.25f;
    } else lg[w] = -1e9f;
    mx = fmaxf(mx, lg[w]);
  }
  float sum = 0.f;
  for (int w = 0; w < 8; ++w) { lg[w] = __expf(lg[w] - mx); sum += lg[w]; }
  float inv = 1.0f / sum;
  float of[16] = {};
  for (int w = 0; w < 8; ++w) {
    int t = s - 2 * w; if (t < 0) t = 0;
    const u16* v = qkv + (size_t)t * 1536 + 1024 + h * 16;
    float p = lg[w] * inv;
    for (int d = 0; d < 16; ++d) of[d] += p * bf2f(v[d]);
  }
  for (int d = 0; d < 16; ++d) o[(size_t)s * D + h * 16 + d] = f2bf(of[d]);
}

__global__ void act_kernel(const u16* __restrict__ g, const u16* __restrict__ u, u16* __restrict__ a, int n) {
  int i = blockIdx.x * blockDim.x + threadIdx.x;
  if (i < n) { float gg = bf2f(g[i]); gg = gg > 0.f ? gg : 0.f; a[i] = f2bf(gg * bf2f(u[i])); }
}

extern "C" void kernel_launch(void* const* d_in, const int* in_sizes, int n_in, void* d_out, int out_size,
                              void* d_ws, size_t ws_size, hipStream_t stream) {
  const u16* x = (const u16*)d_in[0];
  const u16* wqkv = (const u16*)d_in[1];
  const u16* wo = (const u16*)d_in[2];
  const u16* wg = (const u16*)d_in[4];
  const u16* wu = (const u16*)d_in[5];
  const u16* wd = (const u16*)d_in[6];
  u16* out = (u16*)d_out;
  char* ws = (char*)d_ws;
  u16* xn = (u16*)ws;
  u16* qkv = (u16*)(ws + (4 << 20));
  u16* o = (u16*)(ws + (12 << 20));
  u16* h = (u16*)(ws + (16 << 20));
  u16* g = (u16*)(ws + (20 << 20));
  u16* u = (u16*)(ws + (28 << 20));
  u16* a = (u16*)(ws + (36 << 20));
  ln_kernel<<<S / 4, 256, 0, stream>>>(x, xn);
  gemm_kernel<0><<<dim3(1536 / 64, S / 64), 256, 0, stream>>>(xn, wqkv, nullptr, qkv, S, 1536, D);
  attn_kernel<<<S * 32 / 256, 256, 0, stream>>>(qkv, o);
  gemm_kernel<1><<<dim3(D / 64, S / 64), 256, 0, stream>>>(o, wo, x, h, S, D, D);
  ln_kernel<<<S / 4, 256, 0, stream>>>(h, xn);
  gemm_kernel<0><<<dim3((F + 63) / 64, S / 64), 256, 0, stream>>>(xn, wg, nullptr, g, S, F, D);
  gemm_kernel<0><<<dim3((F + 63) / 64, S / 64), 256, 0, stream>>>(xn, wu, nullptr, u, S, F, D);
  act_kernel<<<(S * F + 255) / 256, 256, 0, stream>>>(g, u, a, S * F);
  gemm_kernel<1><<<dim3(D / 64, S / 64), 256, 0, stream>>>(a, wd, h, out, S, D, F);
}
```

```cpp
#include <hip/hip_runtime.h>
#include <stdint.h>

typedef unsigned short u16;
typedef unsigned u32;
typedef unsigned long long u64;
typedef short bf16x8 __attribute__((ext_vector_type(8)));
typedef short bf16x4 __attribute__((ext_vector_type(4)));
typedef float f32x2 __attribute__((ext_vector_type(2)));
typedef float f32x4 __attribute__((ext_vector_type(4)));
typedef float f32x16 __attribute__((ext_vector_type(16)));
typedef unsigned v4u __attribute__((ext_vector_type(4)));
typedef unsigned v2u __attribute__((ext_vector_type(2)));
typedef __bf16 bf2_t __attribute__((ext_vector_type(2)));
#define GAS __attribute__((address_space(1)))
#define LAS __attribute__((address_space(3)))
#define DI __device__ __forceinline__
#define RLX_AGENT __ATOMIC_RELAXED, __HIP_MEMORY_SCOPE_AGENT
typedef GAS u64 gu64;
typedef __amdgpu_buffer_rsrc_t rsrc_t;

constexpr int S = 2048, D = 512, F = 1376;
constexpr u32 O_OFF = 0;
constexpr u32 H_OFF = 2u << 20;
constexpr u32 A_OFF = 4u << 20;
constexpr u32 ST_OFF = 10u << 20;
constexpr u32 FL_OFF = 11u << 20;
constexpr u32 EP_OFF = 12u << 20;
constexpr u32 WS_BYTES = 13u << 20;

constexpr int SLOT = 24576;
constexpr int MISC = 6 * SLOT;
constexpr int LDS_BYTES = 163840;
constexpr int P1_STAGE = 30720;
constexpr int P1_WOFF = 18432;
constexpr int W2_OFF = 3 * P1_STAGE;
constexpr int M_CS = 157696;
constexpr int M_HS = M_CS + 512;
constexpr int M_SQ = M_CS + 1024;
constexpr int M_MR = M_CS + 2048;
constexpr int KV_OFF = 3 * SLOT;
constexpr int M_HK = MISC;
constexpr int M_W4T = MISC + 8704;
constexpr int HK_PITCH = 136;

struct Params {
  const u16* x; const u16* wqkv; const u16* wo; const u16* wg; const u16* wu; const u16* wd;
  u16* out; unsigned char* ws;
};

template <int I> struct IC { static constexpr int v = I; };
template <int I, int N, class Fn> DI void sfor(Fn&& f) { if constexpr (I < N) { f(IC<I>{}); sfor<I + 1, N>(f); } }

DI u32 pack2(float a, float b) { f32x2 v = {a, b}; bf2_t r = __builtin_convertvector(v, bf2_t); return __builtin_bit_cast(u32, r); }
DI float bflo(u32 u) { return __builtin_bit_cast(float, u << 16); }
DI float bfhi(u32 u) { return __builtin_bit_cast(float, u & 0xffff0000u); }
DI float asf(u32 u) { return __builtin_bit_cast(float, u); }
DI u32 asu(float f) { return __builtin_bit_cast(u32, f); }
DI f32x4 mfma16(bf16x8 a, bf16x8 b, f32x4 c) { return __builtin_amdgcn_mfma_f32_16x16x32_bf16(a, b, c, 0, 0, 0); }
DI f32x4 mfma16k16(v2u a, v2u b, f32x4 c) { return __builtin_amdgcn_mfma_f32_16x16x16bf16_1k(__builtin_bit_cast(bf16x4, a), __builtin_bit_cast(bf16x4, b), c, 0, 0, 0); }
DI f32x16 mfma32(bf16x8 a, bf16x8 b, f32x16 c) { return __builtin_amdgcn_mfma_f32_32x32x16_bf16(a, b, c, 0, 0, 0); }
DI bf16x8 cat(bf16x4 a, bf16x4 b) { return __builtin_shufflevector(a, b, 0, 1, 2, 3, 4, 5, 6, 7); }
DI bf16x4 trread(LAS unsigned char* L, int off) { return __builtin_amdgcn_ds_read_tr16_b64_v4i16((LAS bf16x4*)(L + off)); }
DI bf16x8 tr2(LAS unsigned char* L, int off, int d) { return cat(trread(L, off), trread(L, off + d)); }
DI bf16x8 ldsb128(LAS unsigned char* L, int off) { return *(LAS bf16x8*)(L + off); }
DI rsrc_t mkrs(const void* p, u32 bytes) { return __builtin_amdgcn_make_buffer_rsrc((void*)p, 0, bytes, 0x00020000); }
DI u32 rfl(u32 v) { return __builtin_amdgcn_readfirstlane(v); }
DI int opq(int v) { asm volatile("" : "+v"(v)); return v; }
DI float xmax16(float v) { auto r = __builtin_amdgcn_permlane16_swap(asu(v), asu(v), false, false); return fmaxf(asf(r[0]), asf(r[1])); }
DI float xmax32(float v) { auto r = __builtin_amdgcn_permlane32_swap(asu(v), asu(v), false, false); return fmaxf(asf(r[0]), asf(r[1])); }
DI float xadd16(float v) { auto r = __builtin_amdgcn_permlane16_swap(asu(v), asu(v), false, false); return asf(r[0]) + asf(r[1]); }
DI float xadd32(float v) { auto r = __builtin_amdgcn_permlane32_swap(asu(v), asu(v), false, false); return asf(r[0]) + asf(r[1]); }
#define BAR() __builtin_amdgcn_s_barrier()
#define SCHED() __builtin_amdgcn_sched_barrier(0)
#define WAITVN(n) asm volatile("s_waitcnt vmcnt(%0)" ::"i"(n) : "memory")
#define WAITL0() asm volatile("s_waitcnt lgkmcnt(0)" ::: "memory")

DI void dma(rsrc_t rs, u32 voff, u32 soff, u32 ldsaddr) {
  asm volatile("s_mov_b32 m0, %0\n\ts_nop 0\n\tbuffer_load_dwordx4 %1, %2, %3 offen lds" ::"s"(ldsaddr), "v"(voff), "s"(rs), "s"(soff) : "memory");
}
template <int IMM> DI void ldsc1(v4u& dst, rsrc_t rs, u32 voff, u32 soff) {
  asm volatile("buffer_load_dwordx4 %0, %1, %2, %3 offen offset:%4 sc1" : "=v"(dst) : "v"(voff), "s"(rs), "s"(soff), "i"(IMM) : "memory");
}
DI void ldsc1_8(v2u& dst, rsrc_t rs, u32 voff, u32 soff) {
  asm volatile("buffer_load_dwordx2 %0, %1, %2, %3 offen sc1" : "=v"(dst) : "v"(voff), "s"(rs), "s"(soff) : "memory");
}
template <int IMM> DI void ld8(v2u& dst, rsrc_t rs, u32 voff, u32 soff) {
  asm volatile("buffer_load_dwordx2 %0, %1, %2, %3 offen offset:%4" : "=v"(dst) : "v"(voff), "s"(rs), "s"(soff), "i"(IMM) : "memory");
}
template <int N> DI void wait_for(v4u& a) { asm volatile("s_waitcnt vmcnt(%1)" : "+v"(a) : "i"(N > 63 ? 63 : N) : "memory"); }
DI void stsc1(rsrc_t rs, u32 off, v4u v) { __builtin_amdgcn_raw_buffer_store_b128(v, rs, off, 0, 16); }
DI void stsc1_8(rsrc_t rs, u32 off, v2u v) { __builtin_amdgcn_raw_buffer_store_b64(v, rs, off, 0, 16); }

#define STAMP(i) do {} while (0)

DI void publish(gu64* fl, int m, u64 tag) {
  BAR();
  if (threadIdx.x < 16) __hip_atomic_store(fl + threadIdx.x * 16 + m, tag, RLX_AGENT);
}
DI void poll(gu64* fl, int m, int nprod, u64 tag) {
  if (threadIdx.x < 64) {
    const int lane = threadIdx.x;
    for (u32 spins = 0; spins < (1u << 20); ++spins) {
      u64 v = tag;
      if (lane < nprod) v = __hip_atomic_load(fl + m * 16 + lane, RLX_AGENT);
      if (__all(v == tag)) break;
      __builtin_amdgcn_s_sleep(1);
    }
    __builtin_amdgcn_fence(__ATOMIC_ACQUIRE, "wavefront");
  }
}

__global__ __launch_bounds__(256, 1) void fused_block(Params p) {
  extern __shared__ __attribute__((aligned(16))) unsigned char smem[];
  LAS unsigned char* L = (LAS unsigned char*)smem;
  const u32 lbase = (u32)(size_t)L;
  const int tid = threadIdx.x, lane = tid & 63, w = (int)rfl(tid >> 6);
  const int b = blockIdx.x, g = (b & 7) * 2 + ((b >> 3) >> 4), m = (b >> 3) & 15;
  const int T0 = g * 128;
  const int tok = lane & 15, q = lane >> 4;
  const int th = m >> 3, ct = m & 7;
  STAMP(0);

  const rsrc_t rsW = mkrs(p.ws, WS_BYTES);
  const rsrc_t rsX = mkrs(p.x, S * D * 2), rsW1 = mkrs(p.wqkv, D * 3 * D * 2);
  gu64* ep = (gu64*)(p.ws + EP_OFF) + g * 16;
  gu64* fl = (gu64*)(p.ws + FL_OFF) + g * 768;
  v2u epv, xres[4];
  ldsc1_8(epv, rsW, 0, EP_OFF + g * 128);
  {
    const u32 vo = (u32)tok * 1024 + q * 8, so = (u32)(T0 + 64 * th + 16 * w) * 1024 + ct * 128;
    ld8<0>(xres[0], rsX, vo, so); ld8<32>(xres[1], rsX, vo, so); ld8<64>(xres[2], rsX, vo, so); ld8<96>(xres[3], rsX, vo, so);
  }

  u32 vx0, vx1, vxh, vw0, vw1, vw2;
  {
    const int r8 = lane >> 3, sl = lane & 7;
    vx0 = r8 * 1024 + ((sl ^ ((lane >> 4) & 7)) * 16);
    vx1 = r8 * 1024 + ((sl ^ ((4 + (lane >> 4)) & 7)) * 16);
    vxh = (w & 1) ? vx1 : vx0;
    auto pat = [&](int j) { const int e = j * 64 + lane, r = e / 12, pc = e % 12, seg = pc >> 2, sub = pc & 3;
      return (u32)(r * 3072 + seg * 1024 + ((sub ^ (((r >> 3) & 1) << 1)) * 16)); };
    vw0 = pat(0); vw1 = pat(1); vw2 = pat(2);
  }
  const u32 halo0 = (u32)(T0 >= 16 ? T0 - 16 : 0);
  const u32 sxh = (halo0 + 8 * (w & 1)) * 1024, sxm = (u32)(T0 + 32 * w) * 1024, sw1 = (u32)(16 * w) * 3072 + m * 64;
  auto p1_piece = [&](int c, int j) {
    const u32 lb = lbase + (c % 5) * P1_STAGE;
    if (j == 0) dma(rsW1, vw0, sw1 + c * (64 * 3072), lb + P1_WOFF + (3 * w) * 1024);
    if (j == 1) dma(rsW1, vw1, sw1 + c * (64 * 3072), lb + P1_WOFF + (3 * w + 1) * 1024);
    if (j == 2) dma(rsW1, vw2, sw1 + c * (64 * 3072), lb + P1_WOFF + (3 * w + 2) * 1024);
    if (j == 3) dma(rsX, vxh, sxh + c * 128, lb + (w & 1) * 1024);
    if (j == 4) dma(rsX, vx0, sxm + c * 128, lb + (2 + 4 * w) * 1024);
    if (j == 5) dma(rsX, vx1, sxm + c * 128 + 8192, lb + (3 + 4 * w) * 1024);
    if (j == 6) dma(rsX, vx0, sxm + c * 128 + 16384, lb + (4 + 4 * w) * 1024);
    if (j == 7) dma(rsX, vx1, sxm + c * 128 + 24576, lb + (5 + 4 * w) * 1024);
  };
  sfor<0, 24>([&](auto i) { p1_piece(i.v >> 3, i.v & 7); });
  const rsrc_t rsW2 = mkrs(p.wo, D * D * 2);
  const u32 v2 = (lane >> 3) * 1024 + (((lane & 7) ^ (((lane >> 4) & 3) << 1)) * 16);
  auto w2_piece = [&](int i) { dma(rsW2, v2, (u32)(16 * w + i) * 8192 + ct * 128, lbase + W2_OFF + (16 * w + i) * 1024); };

  f32x4 aq[2][2] = {}, ak[2][2] = {}, av[2][2] = {};
  f32x4 ah = {}, ae = {};
  f32x4 asum[2] = {}, agr[2] = {};
  const bf16x8 ones = {0x3F80, 0x3F80, 0x3F80, 0x3F80, 0x3F80, 0x3F80, 0x3F80, 0x3F80};
  int xa[2], wb[2], wbh;
  {
    const int qp = (lane & 15) >> 2, pp = lane & 3;
#pragma unroll
    for (int s2 = 0; s2 < 2; ++s2) xa[s2] = tok * 128 + (((4 * s2 + q) ^ (tok >> 1)) * 16);
#pragma unroll
    for (int hd = 0; hd < 2; ++hd) wb[hd] = P1_WOFF + (8 * q + qp) * 192 + (((2 * hd + (pp >> 1)) ^ ((q & 1) << 1)) * 16) + (pp & 1) * 8;
    wbh = P1_WOFF + (8 * q + qp) * 192 + (((2 * (w & 1) + (pp >> 1)) ^ ((q & 1) << 1)) * 16) + (pp & 1) * 8 + (1 + (w >> 1)) * 64;
  }
  struct P1F { bf16x8 xt[2], xh; bf16x4 w[6][2], wh[2]; };
  struct P1B { int bo, bx, b0, b1, bh; };
  auto p1_bases = [&](int step) {
    const int sb = ((step >> 1) % 5) * P1_STAGE, s2 = step & 1;
    P1B b;
    b.bo = opq(xa[s2] + sb + (1 + 2 * w) * 2048); b.bx = opq(xa[s2] + sb);
    b.b0 = opq(wb[0] + sb + s2 * 6144); b.b1 = opq(wb[1] + sb + s2 * 6144); b.bh = opq(wbh + sb + s2 * 6144);
    return b;
  };
  auto p1_rd = [&](P1F& f, const P1B& b, int i) {
    if (i == 0) f.xt[0] = ldsb128(L, b.bo);
    else if (i <= 12) { const int j = i - 1, t = j >> 1, hf = j & 1;
      const int seg = t % 3, hd = t / 3;
      f.w[2 * seg + hd][hf] = trread(L, (hd ? b.b1 : b.b0) + seg * 64 + hf * 768); }
    else if (i == 13) f.xt[1] = ldsb128(L, b.bo + 2048);
    else if (i == 14) f.xh = ldsb128(L, b.bx);
    else f.wh[i - 15] = trread(L, b.bh + (i - 15) * 768);
  };
  auto p1_vmem = [&](int h, int k) {
    if (h == 0) p1_piece(3, k);
    else if (h == 1) p1_piece(4, k);
    else if (h >= 3 && h <= 8) { if ((k & 1) == 0) p1_piece(5 + ((h - 3) >> 1), ((h - 3) & 1) * 4 + (k >> 1)); }
    else if (h >= 11 && h <= 14) { if ((k & 1) == 0) w2_piece((h - 11) * 4 + (k >> 1)); }
  };
  auto p1_step = [&](auto hc, P1F& f, P1F& n) {
    constexpr int h = hc.v;
    if constexpr (h & 1) {
      constexpr int c = h >> 1;
      if constexpr (c < 7) {
        WAITL0();
        WAITVN(c <= 4 ? 16 : 8);
        BAR();
      }
    }
    P1B b;
    if constexpr (h < 15) b = p1_bases(h + 1);
    bf16x8 xhb;
    sfor<0, 17>([&](auto jc) {
      constexpr int j = jc.v;
      if constexpr (j < 16) {
        constexpr int t = j >> 3, k = j & 7;
        if constexpr (k < 6) { constexpr int seg = k % 3, hd = k / 3;
          const bf16x8 wf = cat(f.w[2 * seg + hd][0], f.w[2 * seg + hd][1]);
          if constexpr (seg == 0) aq[t][hd] = mfma16(wf, f.xt[t], aq[t][hd]);
          if constexpr (seg == 1) ak[t][hd] = mfma16(wf, f.xt[t], ak[t][hd]);
          if constexpr (seg == 2) av[t][hd] = mfma16(f.xt[t], wf, av[t][hd]);
        } else if constexpr (k == 6) asum[t] = mfma16(ones, f.xt[t], asum[t]);
        else agr[t] = mfma16(f.xt[t], f.xt[t], agr[t]);
      } else ah = mfma16(cat(f.wh[0], f.wh[1]), xhb, ah);
      if constexpr (h < 15) p1_rd(n, b, j);
      if constexpr (j == 14) {
        v4u xh = __builtin_bit_cast(v4u, f.xh);
#pragma unroll
        for (int d = 0; d < 4; ++d) xh[d] = tok == 0 ? 0x3F803F80u : xh[d];
        xhb = __builtin_bit_cast(bf16x8, xh);
      }
      if constexpr ((j & 1) == 1 && j < 16) p1_vmem(h, j >> 1);
      SCHED();
    });
    if (w == 0) ae = mfma16(cat(f.w[0][0], f.w[0][1]), xhb, ae);
    else if (w == 1) ae = mfma16(cat(f.w[1][0], f.w[1][1]), xhb, ae);
    else if (w == 2) ae = mfma16(ones, xhb, ae);
    else ae = mfma16(xhb, xhb, ae);
    SCHED();
  };
  P1F fa, fb;
  asm volatile("s_waitcnt vmcnt(16)" : "+v"(epv), "+v"(xres[0]), "+v"(xres[1]), "+v"(xres[2]), "+v"(xres[3])::"memory");
  BAR();
  const u64 tag = (((u64)rfl(epv.y) << 32) | rfl(epv.x)) + 1;
  {
    const P1B b = p1_bases(0);
    sfor<0, 17>([&](auto j) { p1_rd(fa, b, j.v); });
  }
  sfor<0, 8>([&](auto c) { p1_step(IC<2 * c.v>{}, fa, fb); p1_step(IC<2 * c.v + 1>{}, fb, fa); });
  STAMP(1);
  {
    LAS float* cs_ = (LAS float*)(L + M_CS);
    LAS float* hs = (LAS float*)(L + M_HS);
    LAS float* sq = (LAS float*)(L + M_SQ);
    LAS float* mr = (LAS float*)(L + M_MR);
    const bool diag = q == (tok >> 2);
    const int r3 = tok & 3;
    auto pick = [&](f32x4 v) { return r3 == 0 ? v[0] : (r3 == 1 ? v[1] : (r3 == 2 ? v[2] : v[3])); };
    if (tok == 0) {
      *(LAS f32x4*)(cs_ + 32 + 16 * w + 4 * q) = ah;
      if (w < 2) *(LAS f32x4*)(cs_ + 16 * w + 4 * q) = ae;
    }
    if (w == 2 && q == 0) hs[tok] = ae[0];
    if (diag) {
      if (w == 3) hs[16 + tok] = pick(ae);
      sq[(2 * w) * 16 + tok] = pick(agr[0]);
      sq[(2 * w + 1) * 16 + tok] = pick(agr[1]);
    }
    WAITL0();
    BAR();
    float mean[2], rstd[2];
#pragma unroll
    for (int t = 0; t < 2; ++t) {
      mean[t] = asum[t][0] * (1.0f / D);
      const float var = fmaxf(__builtin_fmaf(-mean[t], mean[t], sq[(2 * w + t) * 16 + tok] * (1.0f / D)), 0.f);
      rstd[t] = __builtin_amdgcn_rsqf(var + 1e-5f);
      if (q == 0) { mr[(2 * w + t) * 32 + tok] = mean[t]; mr[(2 * w + t) * 32 + 16 + tok] = rstd[t]; }
    }
    f32x4 cs[4]; float csv[2];
#pragma unroll
    for (int t = 0; t < 4; ++t) cs[t] = *(LAS f32x4*)(cs_ + t * 16 + 4 * q);
#pragma unroll
    for (int hd = 0; hd < 2; ++hd) csv[hd] = cs_[64 + 16 * hd + tok];
    LAS unsigned char* kv = L + KV_OFF;
    {
      const float mu = hs[tok] * (1.0f / D), rs = __builtin_amdgcn_rsqf(fmaxf(__builtin_fmaf(-mu, mu, hs[16 + tok] * (1.0f / D)), 0.f) + 1e-5f);
      const f32x4 c = *(LAS f32x4*)(cs_ + 32 + 16 * w + 4 * q);
      const u32 o0 = pack2(rs * __builtin_fmaf(-mu, c[0], ah[0]), rs * __builtin_fmaf(-mu, c[1], ah[1]));
      const u32 o1 = pack2(rs * __builtin_fmaf(-mu, c[2], ah[2]), rs * __builtin_fmaf(-mu, c[3], ah[3]));
      if (w < 2) *(LAS v2u*)(kv + w * 512 + lane * 8) = v2u{o0, o1};
      else {
        LAS u16* vv = (LAS u16*)(kv + w * 512) + (4 * q + 16 * (tok >> 2)) * 4 + (tok & 3);
        vv[0] = (u16)o0; vv[4] = (u16)(o0 >> 16); vv[8] = (u16)o1; vv[12] = (u16)(o1 >> 16);
      }
    }
    v2u qp[2][2], kp[2][2], vp[2][2];
    WAITL0();
#pragma unroll
    for (int t = 1; t >= 0; --t) {
      const f32x4 mv = *(LAS f32x4*)(mr + (2 * w + t) * 32 + 4 * q), rv = *(LAS f32x4*)(mr + (2 * w + t) * 32 + 16 + 4 * q);
#pragma unroll
      for (int hd = 0; hd < 2; ++hd) {
        float a[4], k4[4], v4[4];
#pragma unroll
        for (int r = 0; r < 4; ++r) {
          a[r] = __builtin_fmaf(-mean[t], cs[hd][r], aq[t][hd][r]);
          k4[r] = rstd[t] * __builtin_fmaf(-mean[t], cs[2 + hd][r], ak[t][hd][r]);
          v4[r] = rv[r] * __builtin_fmaf(-mv[r], csv[hd], av[t][hd][r]);
        }
        qp[t][hd] = v2u{pack2(a[0], a[1]), pack2(a[2], a[3])};
        kp[t][hd] = v2u{pack2(k4[0], k4[1]), pack2(k4[2], k4[3])};
        vp[t][hd] = v2u{pack2(v4[0], v4[1]), pack2(v4[2], v4[3])};
      }
      if (t == 1 && w < 3) {
#pragma unroll
        for (int hd = 0; hd < 2; ++hd) {
          *(LAS v2u*)(kv + (w + 1) * 2048 + hd * 512 + lane * 8) = kp[1][hd];
          *(LAS v2u*)(kv + (w + 1) * 2048 + (2 + hd) * 512 + lane * 8) = vp[1][hd];
        }
      }
    }
    constexpr float SC = 0.25f * 1.4426950408889634f, NEG = -3.0e38f;
    float bias0[4], bias1[4];
#pragma unroll
    for (int r = 0; r < 4; ++r) {
      const int d0 = tok + 16 - (4 * q + r), d1 = tok - (4 * q + r);
      bias0[r] = ((d0 & 1) == 0 && d0 <= 14) ? 0.f : NEG;
      bias1[r] = ((d1 & 1) == 0 && d1 >= 0 && d1 <= 14) ? 0.f : NEG;
    }
    const v2u ones2 = {0x3F803F80u, 0x3F803F80u};
    auto attend = [&](int t, int hd, v2u kA, v2u vA, bool prev_ok) {
      const float sc = SC * rstd[t];
      f32x4 s0 = mfma16k16(kA, qp[t][hd], f32x4{});
      f32x4 s1 = mfma16k16(kp[t][hd], qp[t][hd], f32x4{});
      float e[8];
#pragma unroll
      for (int r = 0; r < 4; ++r) {
        e[r] = __builtin_fmaf(s0[r], sc, prev_ok ? bias0[r] : NEG);
        e[4 + r] = __builtin_fmaf(s1[r], sc, bias1[r]);
      }
      float mx = fmaxf(fmaxf(fmaxf(e[0], e[1]), fmaxf(e[2], e[3])), fmaxf(fmaxf(e[4], e[5]), fmaxf(e[6], e[7])));
      mx = xmax32(xmax16(mx));
#pragma unroll
      for (int r = 0; r < 8; ++r) e[r] = __builtin_amdgcn_exp2f(e[r] - mx);
      const v2u p0 = {pack2(e[0], e[1]), pack2(e[2], e[3])}, p1 = {pack2(e[4], e[5]), pack2(e[6], e[7])};
      f32x4 sm = mfma16k16(ones2, p0, f32x4{});
      sm = mfma16k16(ones2, p1, sm);
      f32x4 o = mfma16k16(vA, p0, f32x4{});
      o = mfma16k16(vp[t][hd], p1, o);
      const float inv = __builtin_amdgcn_rcpf(sm[0]);
      return v2u{pack2(o[0] * inv, o[1] * inv), pack2(o[2] * inv, o[3] * inv)};
    };
    {
      const v2u o0 = attend(1, 0, kp[0][0], vp[0][0], true), o1 = attend(1, 1, kp[0][1], vp[0][1], true);
      stsc1(rsW, O_OFF + (u32)(((g * 8 + 2 * w + 1) * 16 + m) * 1024 + lane * 16), v4u{o0.x, o0.y, o1.x, o1.y});
    }
    WAITL0();
    BAR();
    {
      v2u kprev[2], vprev[2];
#pragma unroll
      for (int hd = 0; hd < 2; ++hd) {
        kprev[hd] = *(LAS v2u*)(kv + w * 2048 + hd * 512 + lane * 8);
        vprev[hd] = *(LAS v2u*)(kv + w * 2048 + (2 + hd) * 512 + lane * 8);
      }
      const bool ok = T0 + 32 * w > 0;
      const v2u o0 = attend(0, 0, kprev[0], vprev[0], ok), o1 = attend(0, 1, kprev[1], vprev[1], ok);
      stsc1(rsW, O_OFF + (u32)(((g * 8 + 2 * w) * 16 + m) * 1024 + lane * 16), v4u{o0.x, o0.y, o1.x, o1.y});
    }
  }
  STAMP(2);
  const rsrc_t rsG = mkrs(p.wg, D * F * 2), rsU = mkrs(p.wu, D * F * 2), rsD = mkrs(p.wd, F * D * 2);
  const u32 v30 = (lane / 12) * (F * 2) + (lane % 12) * 16, v31 = ((64 + lane) / 12) * (F * 2) + ((64 + lane) % 12) * 16,
            v32 = ((128 + lane) / 12) * (F * 2) + ((128 + lane) % 12) * 16;
  const u32 s3 = (u32)(16 * w) * (F * 2) + m * 192;
  auto w3_piece = [&](int c, int slot, int j) {
    const u32 so = s3 + (u32)(64 * c) * (F * 2), lb = lbase + slot * SLOT + 3 * w * 1024 + (j >= 3 ? 12288 : 0) + (j % 3) * 1024;
    const u32 vo = j % 3 == 0 ? v30 : (j % 3 == 1 ? v31 : v32);
    if (j >= 3) dma(rsU, vo, so, lb); else dma(rsG, vo, so, lb);
  };
  auto w3_issue = [&](int c, int slot) { sfor<0, 6>([&](auto j) { w3_piece(c, slot, j.v); }); };
  WAITVN(0);
  publish(fl, m, tag);
  STAMP(3);
  w3_issue(0, 0); w3_issue(1, 1); w3_issue(2, 2);

  {
    bf16x8 w2[16][4];
    {
      const int qp_ = (lane & 15) >> 2, pp = lane & 3;
      const int row = 4 * q + qp_, f = ((row >> 1) & 3) << 1;
      int a[4];
#pragma unroll
      for (int n = 0; n < 4; ++n) a[n] = opq(W2_OFF + row * 128 + (((2 * n + (pp >> 1)) ^ f) * 16) + (pp & 1) * 8);
#pragma unroll
      for (int s = 0; s < 16; ++s)
#pragma unroll
        for (int n = 0; n < 4; ++n) w2[s][n] = tr2(L, a[n] + s * 4096, 2048);
    }
    WAITL0();
    poll(fl, m, 16, tag);
    BAR();
    STAMP(4);
    v4u of[16];
    const u32 ob = O_OFF + (u32)((g * 8 + 4 * th + w) * 16) * 1024;
    sfor<0, 16>([&](auto i) { ldsc1<(i.v & 3) * 1024>(of[i.v], rsW, lane * 16, ob + (i.v >> 2) * 4096); });
    f32x4 acc[4] = {};
    sfor<0, 16>([&](auto s) {
      wait_for<15 - s.v>(of[s.v]);
      const bf16x8 bfr = __builtin_bit_cast(bf16x8, of[s.v]);
#pragma unroll
      for (int n = 0; n < 4; ++n) acc[n] = mfma16(w2[s.v][n], bfr, acc[n]);
    });
    STAMP(5);
    float s = 0.f, ss = 0.f;
    const int tl = 16 * w + tok;
    const int t32 = g * 4 + 2 * th + (w >> 1);
    const u32 hoff = H_OFF + (u32)((t32 * 32 + 4 * ct) * 1024 + (16 * (w & 1) + tok + 32 * (q >> 1)) * 16 + (q & 1) * 8);
#pragma unroll
    for (int n = 0; n < 4; ++n) {
      const u32 h01 = pack2(bflo(xres[n].x) + acc[n][0], bfhi(xres[n].x) + acc[n][1]);
      const u32 h23 = pack2(bflo(xres[n].y) + acc[n][2], bfhi(xres[n].y) + acc[n][3]);
      const v2u hp = {h01, h23};
      stsc1_8(rsW, hoff + n * 1024, hp);
      *(LAS v2u*)(L + M_HK + tl * HK_PITCH + (16 * n + 4 * q) * 2) = hp;
      const float a0 = bflo(h01), a1 = bfhi(h01), a2 = bflo(h23), a3 = bfhi(h23);
      s += (a0 + a1) + (a2 + a3);
      ss = __builtin_fmaf(a0, a0, ss); ss = __builtin_fmaf(a1, a1, ss); ss = __builtin_fmaf(a2, a2, ss); ss = __builtin_fmaf(a3, a3, ss);
    }
    s = xadd32(xadd16(s)); ss = xadd32(xadd16(ss));
    if (q == 0) stsc1_8(rsW, ST_OFF + (u32)((ct * S + T0 + 64 * th + tl) * 8), v2u{asu(s), asu(ss)});
  }
  WAITVN(0);
  publish(fl + 256, m, tag);
  STAMP(6);
  w3_issue(3, 3); w3_issue(4, 4); w3_issue(5, 5);

  const u32 v4s = (lane >> 3) * 1024 + (((lane & 7) ^ (((lane >> 4) & 1) << 2)) * 16);
  const u32 s4 = (u32)(24 * w) * 1024 + ct * 128;
  auto w4_piece = [&](int part, int slot, int j) {
    dma(rsD, v4s, s4 + (u32)(part * 96 + (j >= 3 ? 688 : 0) + (j % 3) * 8) * 1024, lbase + slot * SLOT + 3 * w * 1024 + (j >= 3 ? 12288 : 0) + (j % 3) * 1024);
  };
  auto w4_issue = [&](int part, int slot) { sfor<0, 6>([&](auto j) { w4_piece(part, slot, j.v); }); };
  {
    poll(fl + 256, m, 16, tag);
    BAR();
    STAMP(7);
    const int l32 = lane & 31, hh = lane >> 5;
    const int t32 = g * 4 + w;
    v2u st[4];
#pragma unroll
    for (int i = 0; i < 4; ++i) ldsc1_8(st[i], rsW, (u32)(((hh * 4 + i) * S + l32) * 8), ST_OFF + (u32)(t32 * 32) * 8);
    v4u hf[32];
    const u32 hb = H_OFF + (u32)(t32 * 32) * 1024;
    auto ldh = [&](auto i) { ldsc1<(i.v & 3) * 1024>(hf[i.v], rsW, lane * 16, hb + (i.v >> 2) * 4096); };
    sfor<0, 16>(ldh);
    const int gq = lane >> 4, qp_ = (lane & 15) >> 2, pp = lane & 3;
    const int wa = (8 * (gq >> 1) + qp_) * 192 + (gq & 1) * 32 + pp * 8;
    bf16x8 wA[6], wB[6];
    auto w3_read1 = [&](bf16x8& dst, int base, int i) { dst = tr2(L, base + (i & 1) * 12288 + (i >> 1) * 64, 768); };
    {
      const int base = opq(wa);
#pragma unroll
      for (int i = 0; i < 6; ++i) w3_read1(wA[i], base, i);
    }
    asm volatile("s_waitcnt vmcnt(16)" : "+v"(st[0]), "+v"(st[1]), "+v"(st[2]), "+v"(st[3])::"memory");
    float rs, nb;
    {
      float s = (asf(st[0].x) + asf(st[1].x)) + (asf(st[2].x) + asf(st[3].x)), ss = (asf(st[0].y) + asf(st[1].y)) + (asf(st[2].y) + asf(st[3].y));
      s = xadd32(s); ss = xadd32(ss);
      const float mu = s * (1.0f / D);
      rs = __builtin_amdgcn_rsqf(fmaxf(__builtin_fmaf(-mu, mu, ss * (1.0f / D)), 0.f) + 1e-5f);
      nb = -mu * rs;
    }
    auto ln = [&](v4u h, int d) { return pack2(__builtin_fmaf(bflo(h[d]), rs, nb), __builtin_fmaf(bfhi(h[d]), rs, nb)); };
    v4u yA, yB;
    wait_for<15>(hf[0]);
#pragma unroll
    for (int d = 0; d < 4; ++d) yA[d] = ln(hf[0], d);
    f32x16 acc[6] = {};
    struct Cnt {
      static constexpr int dmas(int s) { return s <= 4 ? 0 : 6 * ((s >> 2) - 1) + (2 * (s & 3) < 6 ? 2 * (s & 3) : 6); }
      static constexpr int issued(int s) { return 16 + (s < 16 ? s : 16) + dmas(s); }
      static constexpr int pos(int k) { return k < 16 ? k : issued(k - 16); }
      static constexpr int batch_end(int b) { return issued(4 * (b + 1) + 3) - 1 + 0; }
    };
    auto step = [&](auto sc, bf16x8(&wc)[6], bf16x8(&wn)[6], v4u& yc, v4u& yn) {
      constexpr int s = sc.v, c1 = (s + 1) >> 2;
      if constexpr (s + 1 < 32) wait_for<Cnt::issued(s) - Cnt::pos(s + 1) - 1>(hf[s + 1]);
      if constexpr (s >= 4 && (s & 3) == 0) {
        WAITL0();
        if constexpr (s == 20) WAITVN(Cnt::issued(20) - Cnt::issued(8));
        if constexpr (s == 24) WAITVN(Cnt::issued(24) - Cnt::issued(12));
        BAR();
      }
      const int base = opq(wa + (c1 % 6) * SLOT + (16 * ((s + 1) & 3)) * 192);
      const bf16x8 yb = __builtin_bit_cast(bf16x8, yc);
      sfor<0, 6>([&](auto ic) {
        constexpr int i = ic.v;
        acc[i] = mfma32(wc[i], yb, acc[i]);
        if constexpr (s + 1 < 32) {
          w3_read1(wn[i], base, i);
          if constexpr (i < 4) yn[i] = ln(hf[s + 1], i);
        }
        if constexpr (i == 1 && s < 16) ldh(IC<(16 + s) & 31>{});
        if constexpr (i >= 4 && s >= 4 && (s & 3) < 3) {
          constexpr int bt = (s >> 2) - 1, j = 2 * (s & 3) + (i - 4);
          if constexpr (bt < 2) w3_piece(6 + bt, bt % 6, j); else w4_piece(bt - 2, bt % 6, j);
        }
        SCHED();
      });
    };
    sfor<0, 16>([&](auto k) { step(IC<2 * k.v>{}, wA, wB, yA, yB); step(IC<2 * k.v + 1>{}, wB, wA, yB, yA); });
    STAMP(8);
    WAITL0(); BAR();
#pragma unroll
    for (int i = 0; i < 3; ++i) {
      const int f0 = 96 * m + 32 * i;
      if (f0 < F) {
#pragma unroll
        for (int s = 0; s < 2; ++s) {
          v4u o;
#pragma unroll
          for (int d = 0; d < 4; ++d) {
            const int r = 8 * s + 2 * d;
            o[d] = pack2(fmaxf(acc[2 * i][r], 0.f) * acc[2 * i + 1][r], fmaxf(acc[2 * i][r + 1], 0.f) * acc[2 * i + 1][r + 1]);
          }
          stsc1(rsW, A_OFF + (u32)((t32 * 86 + f0 / 16 + s) * 1024 + lane * 16), o);
        }
      }
    }
    WAITVN(0);
  }
  publish(fl + 512, m, tag);
  {
    w4_issue(5, 1);
    dma(rsD, v4s, (u32)((w >> 1) * 688 + 672 + (w & 1) * 8) * 1024 + ct * 128, lbase + M_W4T + w * 1024);
  }
  STAMP(9);

  {
    poll(fl + 512, m, 15, tag);
    WAITVN(0);
    BAR();
    STAMP(10);
    const int mh = w & 1, kh = w >> 1;
    const int l32 = lane & 31, hh = lane >> 5;
    const int t32 = g * 4 + 2 * th + mh;
    v4u af[43];
    const u32 ab = A_OFF + (u32)(t32 * 86 + kh * 43) * 1024;
    auto lda = [&](auto i) { ldsc1<(i.v & 3) * 1024>(af[i.v], rsW, lane * 16, ab + (i.v >> 2) * 4096); };
    sfor<0, 20>(lda);
    f32x16 acc[2] = {};
    const int gq = lane >> 4, qp_ = (lane & 15) >> 2, pp = lane & 3;
    int wa[2];
#pragma unroll
    for (int n = 0; n < 2; ++n) wa[n] = (4 * (gq >> 1) + qp_) * 128 + ((n ^ ((qp_ >> 1) & 1)) * 64) + (gq & 1) * 32 + pp * 8;
    bf16x8 wA[2], wB[2];
    auto w4_read = [&](bf16x8(&dst)[2], auto sc) {
      constexpr int s = sc.v;
      const int off = s < 42 ? ((s / 6 + 2) % 6) * SLOT + kh * 12288 + (s % 6) * 2048 : M_W4T + kh * 2048;
#pragma unroll
      for (int n = 0; n < 2; ++n) dst[n] = tr2(L, opq(wa[n] + off), 1024);
    };
    w4_read(wA, IC<0>{});
    struct Cnt {
      static constexpr int issued(int s) { return 20 + (s < 23 ? s : 23) + (s <= 6 ? 0 : (s - 6 < 6 ? s - 6 : 6)); }
      static constexpr int pos(int k) { return k < 20 ? k : issued(k - 20); }
    };
    auto step = [&](auto sc, bf16x8(&wc)[2], bf16x8(&wn)[2]) {
      constexpr int s = sc.v;
      wait_for<Cnt::issued(s) - Cnt::pos(s) - 1>(af[s]);
      if constexpr (s == 6) { WAITL0(); BAR(); }
      if constexpr (s == 30) { WAITVN(0); BAR(); }
      if constexpr (s + 1 < 43) w4_read(wn, IC<s + 1>{});
      const bf16x8 bfr = __builtin_bit_cast(bf16x8, af[s]);
      acc[0] = mfma32(wc[0], bfr, acc[0]);
      if constexpr (s < 23) lda(IC<(s < 23 ? 20 + s : 0)>{});
      SCHED();
      acc[1] = mfma32(wc[1], bfr, acc[1]);
      if constexpr (s >= 6 && s < 12) w4_piece(6, 2, s - 6);
      SCHED();
    };
    sfor<0, 21>([&](auto k) { step(IC<2 * k.v>{}, wA, wB); step(IC<2 * k.v + 1>{}, wB, wA); });
    step(IC<42>{}, wA, wB);
    STAMP(11);
    LAS f32x4* red = (LAS f32x4*)(L + 3 * SLOT);
    {
      const f32x16 snd = kh ? acc[0] : acc[1];
#pragma unroll
      for (int gg = 0; gg < 4; ++gg) red[(w * 4 + gg) * 64 + lane] = f32x4{snd[4 * gg], snd[4 * gg + 1], snd[4 * gg + 2], snd[4 * gg + 3]};
    }
    if (m == 0 && tid == 0) __hip_atomic_store(ep, tag, RLX_AGENT);
    WAITL0(); BAR();
    {
      const f32x16 keep = kh ? acc[1] : acc[0];
      const int pw = w ^ 2;
      GAS u16* orow = (GAS u16*)p.out + (size_t)(T0 + 64 * th + 32 * mh + l32) * D + 64 * ct + 32 * kh + 4 * hh;
#pragma unroll
      for (int gg = 0; gg < 4; ++gg) {
        const f32x4 o = red[(pw * 4 + gg) * 64 + lane];
        const v2u hk = *(LAS v2u*)(L + M_HK + (32 * mh + l32) * HK_PITCH + (32 * kh + 8 * gg + 4 * hh) * 2);
        v2u r;
        r.x = pack2(bflo(hk.x) + (keep[4 * gg] + o[0]), bfhi(hk.x) + (keep[4 * gg + 1] + o[1]));
        r.y = pack2(bflo(hk.y) + (keep[4 * gg + 2] + o[2]), bfhi(hk.y) + (keep[4 * gg + 3] + o[3]));
        *(GAS v2u*)(orow + 8 * gg) = r;
      }
    }
  }
  STAMP(12);
}

extern "C" void kernel_launch(void* const* d_in, const int* in_sizes, int n_in, void* d_out, int out_size,
                              void* d_ws, size_t ws_size, hipStream_t stream) {
  Params p;
  p.x = (const u16*)d_in[0]; p.wqkv = (const u16*)d_in[1]; p.wo = (const u16*)d_in[2];
  p.wg = (const u16*)d_in[4]; p.wu = (const u16*)d_in[5]; p.wd = (const u16*)d_in[6];
  p.out = (u16*)d_out; p.ws = (unsigned char*)d_ws;
  (void)hipFuncSetAttribute((const void*)fused_block, hipFuncAttributeMaxDynamicSharedMemorySize, LDS_BYTES);
  fused_block<<<256, 256, LDS_BYTES, stream>>>(p);
}
```

```cpp
#include <hip/hip_runtime.h>
#include <stdint.h>

typedef unsigned short u16;
typedef unsigned u32;
typedef unsigned long long u64;
typedef short bf16x8 __attribute__((ext_vector_type(8)));
typedef short bf16x4 __attribute__((ext_vector_type(4)));
typedef float f32x2 __attribute__((ext_vector_type(2)));
typedef float f32x4 __attribute__((ext_vector_type(4)));
typedef float f32x16 __attribute__((ext_vector_type(16)));
typedef unsigned v4u __attribute__((ext_vector_type(4)));
typedef unsigned v2u __attribute__((ext_vector_type(2)));
typedef __bf16 bf2_t __attribute__((ext_vector_type(2)));
#define GAS __attribute__((address_space(1)))
#define LAS __attribute__((address_space(3)))
#define DI __device__ __forceinline__
#define RLX_AGENT __ATOMIC_RELAXED, __HIP_MEMORY_SCOPE_AGENT
typedef GAS u64 gu64;
typedef __amdgpu_buffer_rsrc_t rsrc_t;

constexpr int S = 2048, D = 512, F = 1376;
constexpr u32 O_OFF = 0;
constexpr u32 H_OFF = 2u << 20;
constexpr u32 A_OFF = 4u << 20;
constexpr u32 ST_OFF = 10u << 20;
constexpr u32 ST1_OFF = (10u << 20) + (512u << 10);
constexpr u32 FL_OFF = 11u << 20;
constexpr u32 EP_OFF = 12u << 20;
constexpr u32 WS_BYTES = 13u << 20;

constexpr int SLOT = 24576;
constexpr int MISC = 6 * SLOT;
constexpr int LDS_BYTES = 163840;
constexpr int P1_STAGE = 30720;
constexpr int P1_WOFF = 18432;
constexpr int W2_OFF = 3 * P1_STAGE;
constexpr int M_CS = 157696;
constexpr int M_MR = M_CS + 1024;
constexpr int KVH_OFF = M_CS + 2048;
constexpr int KV_OFF = 3 * SLOT;
constexpr int M_HK = MISC;
constexpr int M_W4T = MISC + 8704;
constexpr int HK_PITCH = 136;

struct Params {
  const u16* x; const u16* wqkv; const u16* wo; const u16* wg; const u16* wu; const u16* wd;
  u16* out; unsigned char* ws;
};

template <int I> struct IC { static constexpr int v = I; };
template <int I, int N, class Fn> DI void sfor(Fn&& f) { if constexpr (I < N) { f(IC<I>{}); sfor<I + 1, N>(f); } }

DI u32 pack2(float a, float b) { f32x2 v = {a, b}; bf2_t r = __builtin_convertvector(v, bf2_t); return __builtin_bit_cast(u32, r); }
DI float bflo(u32 u) { return __builtin_bit_cast(float, u << 16); }
DI float bfhi(u32 u) { return __builtin_bit_cast(float, u & 0xffff0000u); }
DI float asf(u32 u) { return __builtin_bit_cast(float, u); }
DI u32 asu(float f) { return __builtin_bit_cast(u32, f); }
DI f32x4 mfma16(bf16x8 a, bf16x8 b, f32x4 c) { return __builtin_amdgcn_mfma_f32_16x16x32_bf16(a, b, c, 0, 0, 0); }
DI f32x4 mfma16k16(v2u a, v2u b, f32x4 c) { return __builtin_amdgcn_mfma_f32_16x16x16bf16_1k(__builtin_bit_cast(bf16x4, a), __builtin_bit_cast(bf16x4, b), c, 0, 0, 0); }
DI f32x16 mfma32(bf16x8 a, bf16x8 b, f32x16 c) { return __builtin_amdgcn_mfma_f32_32x32x16_bf16(a, b, c, 0, 0, 0); }
DI bf16x8 cat(bf16x4 a, bf16x4 b) { return __builtin_shufflevector(a, b, 0, 1, 2, 3, 4, 5, 6, 7); }
#pragma clang diagnostic ignored "-Wint-to-pointer-cast"
DI bf16x4 trr(int off) { return __builtin_amdgcn_ds_read_tr16_b64_v4i16((LAS bf16x4*)(u32)off); }
DI bf16x8 tr2(int off, int d) { return cat(trr(off), trr(off + d)); }
DI bf16x8 ldsb(int off) { return *(LAS bf16x8*)(u32)off; }
template <class T> DI LAS T* lp(int off) { return (LAS T*)(u32)off; }
DI rsrc_t mkrs(const void* p, u32 bytes) { return __builtin_amdgcn_make_buffer_rsrc((void*)p, 0, bytes, 0x00020000); }
DI u32 rfl(u32 v) { return __builtin_amdgcn_readfirstlane(v); }
DI int opq(int v) { asm volatile("" : "+v"(v)); return v; }
DI float xmax16(float v) { auto r = __builtin_amdgcn_permlane16_swap(asu(v), asu(v), false, false); return fmaxf(asf(r[0]), asf(r[1])); }
DI float xmax32(float v) { auto r = __builtin_amdgcn_permlane32_swap(asu(v), asu(v), false, false); return fmaxf(asf(r[0]), asf(r[1])); }
DI float xadd16(float v) { auto r = __builtin_amdgcn_permlane16_swap(asu(v), asu(v), false, false); return asf(r[0]) + asf(r[1]); }
DI float xadd32(float v) { auto r = __builtin_amdgcn_permlane32_swap(asu(v), asu(v), false, false); return asf(r[0]) + asf(r[1]); }
#define BAR() __builtin_amdgcn_s_barrier()
#define SCHED() __builtin_amdgcn_sched_barrier(0)
#define WAITVN(n) asm volatile("s_waitcnt vmcnt(%0)" ::"i"(n) : "memory")
#define WAITL0() asm volatile("s_waitcnt lgkmcnt(0)" ::: "memory")

DI void dma(rsrc_t rs, u32 voff, u32 soff, u32 ldsaddr) {
  asm volatile("s_mov_b32 m0, %0\n\ts_nop 0\n\tbuffer_load_dwordx4 %1, %2, %3 offen lds" ::"s"(ldsaddr), "v"(voff), "s"(rs), "s"(soff) : "memory");
}
template <int IMM> DI void ldsc1(v4u& dst, rsrc_t rs, u32 voff, u32 soff) {
  asm volatile("buffer_load_dwordx4 %0, %1, %2, %3 offen offset:%4 sc1" : "=v"(dst) : "v"(voff), "s"(rs), "s"(soff), "i"(IMM) : "memory");
}
DI void ldsc1_8(v2u& dst, rsrc_t rs, u32 voff, u32 soff) {
  asm volatile("buffer_load_dwordx2 %0, %1, %2, %3 offen sc1" : "=v"(dst) : "v"(voff), "s"(rs), "s"(soff) : "memory");
}
DI void ld16(v4u& dst, rsrc_t rs, u32 voff, u32 soff) {
  asm volatile("buffer_load_dwordx4 %0, %1, %2, %3 offen" : "=v"(dst) : "v"(voff), "s"(rs), "s"(soff) : "memory");
}
DI void ld4_keep(u32& dst, rsrc_t rs, u32 voff) { asm volatile("buffer_load_dword %0, %1, %2, 0 offen" : "+v"(dst) : "v"(voff), "s"(rs) : "memory"); }
template <int IMM> DI void ld8(v2u& dst, rsrc_t rs, u32 voff, u32 soff) {
  asm volatile("buffer_load_dwordx2 %0, %1, %2, %3 offen offset:%4" : "=v"(dst) : "v"(voff), "s"(rs), "s"(soff), "i"(IMM) : "memory");
}
template <int N> DI void wait_for(v4u& a) { asm volatile("s_waitcnt vmcnt(%1)" : "+v"(a) : "i"(N > 63 ? 63 : N) : "memory"); }
DI void stsc1(rsrc_t rs, u32 off, v4u v) { __builtin_amdgcn_raw_buffer_store_b128(v, rs, off, 0, 16); }
DI void stsc1_8(rsrc_t rs, u32 off, v2u v) { __builtin_amdgcn_raw_buffer_store_b64(v, rs, off, 0, 16); }

#define STAMP(i) do {} while (0)

DI void publish(gu64* fl, int m, u64 tag) {
  BAR();
  if (threadIdx.x < 16) __hip_atomic_store(fl + threadIdx.x * 16 + m, tag, RLX_AGENT);
}
DI void poll(gu64* fl, int m, int nprod, u64 tag) {
  if (threadIdx.x < 64) {
    const int lane = threadIdx.x;
    for (u32 spins = 0; spins < (1u << 20); ++spins) {
      u64 v = tag;
      if (lane < nprod) v = __hip_atomic_load(fl + m * 16 + lane, RLX_AGENT);
      if (__all(v == tag)) break;
      __builtin_amdgcn_s_sleep(1);
    }
    __builtin_amdgcn_fence(__ATOMIC_ACQUIRE, "wavefront");
  }
}

__global__ __launch_bounds__(512, 2) void fused_block(Params p) {
  extern __shared__ __attribute__((aligned(16))) unsigned char smem[];
  const int tid = threadIdx.x, lane = tid & 63, w8 = (int)rfl(tid >> 6), w = w8 & 3;
  const int b = blockIdx.x, g = (b & 7) * 2 + ((b >> 3) >> 4), m = (b >> 3) & 15;
  const int T0 = g * 128;
  const int tok = lane & 15, q = lane >> 4;
  const int th = m >> 3, ct = m & 7;
  const rsrc_t rsW = mkrs(p.ws, WS_BYTES);
  const rsrc_t rsX = mkrs(p.x, S * D * 2);
  const u32 halo0 = (u32)(T0 >= 16 ? T0 - 16 : 0);
  asm volatile("" ::"s"((u32)(size_t)smem));

  if (w8 >= 4) {
    const rsrc_t rsW1 = mkrs(p.wqkv, D * 3 * D * 2);
    v2u epv;
    v4u srow[3];
    ldsc1_8(epv, rsW, 0, EP_OFF + g * 128);
    sfor<0, 3>([&](auto jc) {
      const int j = jc.v < 2 ? w + 4 * jc.v : 8, R = 9 * m + j;
      const u32 token = R < 16 ? halo0 + R : (u32)(T0 + R - 16);
      ld16(srow[jc.v], rsX, lane * 16, token * 1024);
    });
    u32 vx0, vx1, vxh, vw0, vw1, vw2;
    {
      const int r8 = lane >> 3, sl = lane & 7;
      vx0 = r8 * 1024 + ((sl ^ ((lane >> 4) & 7)) * 16);
      vx1 = r8 * 1024 + ((sl ^ ((4 + (lane >> 4)) & 7)) * 16);
      vxh = (w & 1) ? vx1 : vx0;
      auto pat = [&](int j) { const int e = j * 64 + lane, r = e / 12, pc = e % 12; return (u32)(r * 3072 + (pc >> 2) * 1024 + (pc & 3) * 16); };
      vw0 = pat(0); vw1 = pat(1); vw2 = pat(2);
    }
    const u32 sxh = (halo0 + 8 * (w & 1)) * 1024, sxm = (u32)(T0 + 32 * w) * 1024, sw1 = (u32)(16 * w) * 3072 + m * 64;
    const int rot = (g + m) & 7;
    auto p1_chunk = [&](int cc) {
      const u32 lb = (cc % 5) * P1_STAGE;
      const u32 c = (u32)((cc + rot) & 7);
      dma(rsX, vxh, sxh + c * 128, lb + (w & 1) * 1024);
      dma(rsW1, vw0, sw1 + c * (64 * 3072), lb + P1_WOFF + (3 * w) * 1024);
      dma(rsX, vx0, sxm + c * 128, lb + (2 + 4 * w) * 1024);
      dma(rsW1, vw1, sw1 + c * (64 * 3072), lb + P1_WOFF + (3 * w + 1) * 1024);
      dma(rsX, vx1, sxm + c * 128 + 8192, lb + (3 + 4 * w) * 1024);
      dma(rsW1, vw2, sw1 + c * (64 * 3072), lb + P1_WOFF + (3 * w + 2) * 1024);
      dma(rsX, vx0, sxm + c * 128 + 16384, lb + (4 + 4 * w) * 1024);
      dma(rsX, vx1, sxm + c * 128 + 24576, lb + (5 + 4 * w) * 1024);
    };
    const int qp = (lane & 15) >> 2, pp = lane & 3;
    f32x4 ahA = {}, ahB = {};
    const int xha0 = tok * 128 + ((q ^ (tok >> 1)) * 16), xha1 = tok * 128 + (((4 + q) ^ (tok >> 1)) * 16);
    const int wha = P1_WOFF + (8 * q + qp) * 192 + (1 + (w >> 1)) * 64 + (w & 1) * 32 + pp * 8;
    const int whb = P1_WOFF + (8 * q + qp) * 192 + (w < 2 ? 0 : 128) + (w & 1) * 32 + pp * 8;
    auto halo = [&](int cc) {
      const int sb = (cc % 5) * P1_STAGE;
#pragma unroll
      for (int s2 = 0; s2 < 2; ++s2) {
        v4u xh = __builtin_bit_cast(v4u, ldsb((s2 ? xha1 : xha0) + sb));
        const bf16x8 fa = tr2(wha + sb + s2 * 6144, 768), fb = tr2(whb + sb + s2 * 6144, 768);
#pragma unroll
        for (int d = 0; d < 4; ++d) xh[d] = tok == 0 ? 0x3F803F80u : xh[d];
        const bf16x8 xhb = __builtin_bit_cast(bf16x8, xh);
        ahA = mfma16(fa, xhb, ahA);
        ahB = mfma16(fb, xhb, ahB);
      }
    };
    p1_chunk(0); p1_chunk(1);
    WAITVN(8); BAR();
    p1_chunk(2);
    halo(0);
    WAITVN(8); BAR();
    p1_chunk(3);
    halo(1);
    asm volatile("s_waitcnt vmcnt(32)" : "+v"(epv), "+v"(srow[0]), "+v"(srow[1]), "+v"(srow[2])::"memory");
    const u64 tag = (((u64)rfl(epv.y) << 32) | rfl(epv.x)) + 1;
    const u32 tag32 = (u32)tag ^ (u32)(tag >> 32) ^ 0x9E3779B9u;
    {
      float sv[3], qv[3];
#pragma unroll
      for (int j = 0; j < 3; ++j) {
        float s = 0.f, ss = 0.f;
#pragma unroll
        for (int d = 0; d < 4; ++d) {
          const float a = bflo(srow[j][d]), c = bfhi(srow[j][d]);
          s += a + c; ss = __builtin_fmaf(a, a, ss); ss = __builtin_fmaf(c, c, ss);
        }
        sv[j] = s; qv[j] = ss;
      }
      auto s32 = [&](float a, float c) { auto r = __builtin_amdgcn_permlane32_swap(asu(a), asu(c), false, false); return asf(r[0]) + asf(r[1]); };
      auto s16 = [&](float a, float c) { auto r = __builtin_amdgcn_permlane16_swap(asu(a), asu(c), false, false); return asf(r[0]) + asf(r[1]); };
      const float A0 = s32(sv[0], qv[0]), A1 = s32(sv[1], qv[1]), A2 = s32(sv[2], qv[2]);
      float B = s16(A0, A1), C = s16(A2, A2);
      auto ror = [&](float v, auto n) { return v + asf(__builtin_amdgcn_update_dpp(0, asu(v), 0x120 + n.v, 0xf, 0xf, false)); };
      B = ror(B, IC<8>{}); C = ror(C, IC<8>{}); B = ror(B, IC<4>{}); C = ror(C, IC<4>{}); B = ror(B, IC<2>{}); C = ror(C, IC<2>{}); B = ror(B, IC<1>{}); C = ror(C, IC<1>{});
      const auto rb = __builtin_amdgcn_permlane32_swap(asu(B), asu(B), false, false), rc = __builtin_amdgcn_permlane32_swap(asu(C), asu(C), false, false);
      const float s = lane < 32 ? asf(rb[0]) : asf(rc[0]), ss = lane < 32 ? asf(rb[1]) : asf(rc[1]);
      const float mu = s * (1.0f / D), rs = __builtin_amdgcn_rsqf(fmaxf(__builtin_fmaf(-mu, mu, ss * (1.0f / D)), 0.f) + 1e-5f);
      const int j = q < 2 ? w + 4 * q : 8;
      if (tok == 0 && q < 3) stsc1(rsW, ST1_OFF + (u32)(g * 4096 + (9 * m + j) * 16), v4u{asu(mu), tag32, asu(rs), tag32});
    }
    const rsrc_t rsW2 = mkrs(p.wo, D * D * 2), rsG = mkrs(p.wg, D * F * 2), rsU = mkrs(p.wu, D * F * 2), rsD = mkrs(p.wd, F * D * 2);
    WAITVN(9); BAR();
    p1_chunk(4); p1_chunk(5);
    halo(2);
    u32 touch = 0;
    {
      const u32 wg_ = (u32)(b * 4 + w);
      const u32 o4 = (u32)lane < 4u ? (wg_ * 4u + lane) * 128u : 0xFFFFFF00u, o11 = (u32)lane < 11u ? (wg_ * 11u + lane) * 128u : 0xFFFFFF00u;
      ld4_keep(touch, rsW2, o4); ld4_keep(touch, rsG, o11); ld4_keep(touch, rsU, o11); ld4_keep(touch, rsD, o11);
    }
    WAITVN(21); BAR(); p1_chunk(6); halo(3);
    WAITVN(20); BAR(); p1_chunk(7); halo(4);
    WAITVN(16); BAR(); halo(5);
    v4u sth;
    ldsc1<0>(sth, rsW, ST1_OFF + (u32)(g * 4096 + tok * 16), 0);
    WAITVN(9); BAR();
    const u32 v2 = (lane >> 3) * 1024 + (((lane & 7) ^ (((lane >> 4) & 3) << 1)) * 16);
    auto w2_piece = [&](int i) {
      const int ii = 16 * w + i, ls = ii >> 2;
      dma(rsW2, v2, (u32)(32 * ((ls + 2 * ct) & 15) + 8 * (ii & 3)) * 1024 + ct * 128, W2_OFF + ii * 1024);
    };
    sfor<0, 8>([&](auto i) { w2_piece(i.v); });
    halo(6);
    WAITVN(8); BAR();
    halo(7);
    asm volatile("" : "+v"(sth), "+v"(touch));
    for (u32 spins = 0; spins < (1u << 20); ++spins) {
      if (__all(sth.y == tag32 && sth.w == tag32)) break;
      __builtin_amdgcn_s_sleep(1);
      ldsc1<0>(sth, rsW, ST1_OFF + (u32)(g * 4096 + tok * 16), 0);
      wait_for<0>(sth);
    }
    {
      LAS float* cs_ = lp<float>(M_CS);
      if (tok == 0) {
        *(LAS f32x4*)(cs_ + 32 + 16 * w + 4 * q) = ahA;
        if (w < 2) *(LAS f32x4*)(cs_ + 16 * w + 4 * q) = ahB;
      }
      const float hmu = asf(sth.x), hrs = asf(sth.z);
      float hv[4];
#pragma unroll
      for (int r = 0; r < 4; ++r) {
        const float c = asf(__builtin_amdgcn_update_dpp(0, asu(ahA[r]), 0x150, 0xf, 0xf, false));
        hv[r] = hrs * __builtin_fmaf(-hmu, c, ahA[r]);
      }
      const u32 o0 = pack2(hv[0], hv[1]), o1 = pack2(hv[2], hv[3]);
      if (w < 2) *lp<v2u>(KVH_OFF + w * 512 + (tok + 16 * (((q & 1) << 1) | (q >> 1))) * 8) = v2u{o0, o1};
      else {
        LAS u16* vv = lp<u16>(KVH_OFF + w * 512) + (4 * q + 16 * (tok >> 2)) * 4 + (tok & 3);
        vv[0] = (u16)o0; vv[4] = (u16)(o0 >> 16); vv[8] = (u16)o1; vv[12] = (u16)(o1 >> 16);
      }
    }
    WAITL0();
    const u32 v30 = (lane / 12) * (F * 2) + (lane % 12) * 16, v31 = ((64 + lane) / 12) * (F * 2) + ((64 + lane) % 12) * 16,
              v32 = ((128 + lane) / 12) * (F * 2) + ((128 + lane) % 12) * 16;
    const u32 s3 = (u32)(16 * w) * (F * 2) + m * 192;
    auto w3_chunk = [&](int c) {
      const u32 so = s3 + (u32)(64 * ((c + m) & 7)) * (F * 2), lb = (c % 6) * SLOT + 3 * w * 1024;
      dma(rsG, v30, so, lb); dma(rsG, v31, so, lb + 1024); dma(rsG, v32, so, lb + 2048);
      dma(rsU, v30, so, lb + 12288); dma(rsU, v31, so, lb + 12288 + 1024); dma(rsU, v32, so, lb + 12288 + 2048);
    };
    const u32 v4s = (lane >> 3) * 1024 + (((lane & 7) ^ (((lane >> 4) & 1) << 2)) * 16);
    auto w4_row = [&](int ls) { const int ps = ls + 5 * ct; return (u32)((ps >= 43 ? ps - 43 : ps) * 16); };
    auto w4_part = [&](int part) {
      const u32 lb = ((part + 2) % 6) * SLOT + 3 * w * 1024;
#pragma unroll
      for (int j = 0; j < 3; ++j) {
        const int jj = 3 * w + j;
        const u32 so = (w4_row(6 * part + (jj >> 1)) + (jj & 1) * 8) * 1024 + ct * 128;
        dma(rsD, v4s, so, lb + j * 1024); dma(rsD, v4s, so + 688 * 1024, lb + 12288 + j * 1024);
      }
    };
    BAR();
    sfor<8, 16>([&](auto i) { w2_piece(i.v); });
    BAR();
    w3_chunk(0); w3_chunk(1);
    WAITVN(12);
    BAR();
    w3_chunk(2);
    BAR();
    BAR();
    w3_chunk(3);
    WAITVN(6); BAR();
    w3_chunk(4); w3_chunk(5);
    BAR(); w3_chunk(6);
    WAITVN(18); BAR(); w3_chunk(7);
    WAITVN(18); BAR(); w4_part(0);
    WAITVN(18); BAR(); w4_part(1);
    WAITVN(18); BAR(); w4_part(2);
    WAITVN(18); BAR(); w4_part(3);
    BAR(); w4_part(4);
    BAR();
    w4_part(5);
    dma(rsD, v4s, ((u32)(w >> 1) * 688 + w4_row(42) + (w & 1) * 8) * 1024 + ct * 128, M_W4T + w * 1024);
    BAR();
    WAITVN(7); BAR();
    WAITVN(0); BAR(); w4_part(6);
    WAITVN(0); BAR();
    BAR();
    return;
  }

  STAMP(0);
  gu64* ep = (gu64*)(p.ws + EP_OFF) + g * 16;
  gu64* fl = (gu64*)(p.ws + FL_OFF) + g * 768;
  v2u epv, xres[4];
  ldsc1_8(epv, rsW, 0, EP_OFF + g * 128);
  {
    const u32 vo = (u32)tok * 1024 + q * 8, so = (u32)(T0 + 64 * th + 16 * w) * 1024 + ct * 128;
    ld8<0>(xres[0], rsX, vo, so); ld8<32>(xres[1], rsX, vo, so); ld8<64>(xres[2], rsX, vo, so); ld8<96>(xres[3], rsX, vo, so);
  }

  const int l32 = lane & 31, hh = lane >> 5;
  const int qp = (lane & 15) >> 2, pp = lane & 3;
  u64 tag;
  {
    f32x16 aq = {}, ak = {}, av = {};
    int xa[4], wa1;
#pragma unroll
    for (int s4 = 0; s4 < 4; ++s4) xa[s4] = (16 + 32 * w + l32) * 128 + (((2 * s4 + hh) ^ ((l32 >> 1) & 7)) * 16);
    wa1 = P1_WOFF + (8 * hh + qp) * 192 + (q & 1) * 32 + pp * 8;
    constexpr float SC = 0.25f * 1.4426950408889634f, NEG = -3.0e38f;
    float bias0[4], bias1[4];
#pragma unroll
    for (int r = 0; r < 4; ++r) {
      const int d0 = tok + 16 - (4 * q + r), d1 = tok - (4 * q + r);
      bias0[r] = asf(opq(((d0 & 1) == 0 && d0 <= 14) ? 0 : (int)asu(NEG)));
      bias1[r] = asf(opq(((d1 & 1) == 0 && d1 >= 0 && d1 <= 14) ? 0 : (int)asu(NEG)));
    }
    struct P1F { bf16x8 xf; bf16x4 w[3][2]; };
    auto f_rd = [&](P1F& f, int u, int i) {
      const int sb = ((u >> 2) % 5) * P1_STAGE, s4 = u & 3;
      if (i == 0) f.xf = ldsb(xa[s4] + sb);
      else f.w[(i - 1) >> 1][(i - 1) & 1] = trr(wa1 + sb + s4 * 3072 + ((i - 1) >> 1) * 64 + ((i - 1) & 1) * 768);
    };
    P1F fa, fb;
    v4u st1;
    auto p1_step = [&](auto uc, P1F& f, P1F& n) {
      constexpr int u = uc.v;
      if constexpr ((u & 3) == 2 && u < 28) BAR();
      if constexpr (u == 24) ldsc1<0>(st1, rsW, ST1_OFF + (u32)(g * 4096 + (16 + 32 * w + l32) * 16), 0);
      sfor<0, 3>([&](auto ic) {
        constexpr int i = ic.v;
        const bf16x8 wf = cat(f.w[i][0], f.w[i][1]);
        if constexpr (i == 0) aq = mfma32(wf, f.xf, aq);
        if constexpr (i == 1) ak = mfma32(wf, f.xf, ak);
        if constexpr (i == 2) av = mfma32(f.xf, wf, av);
        if constexpr (u < 31) {
          if constexpr (i == 0) { f_rd(n, u + 1, 0); f_rd(n, u + 1, 1); f_rd(n, u + 1, 2); }
          if constexpr (i == 1) { f_rd(n, u + 1, 3); f_rd(n, u + 1, 4); }
          if constexpr (i == 2) { f_rd(n, u + 1, 5); f_rd(n, u + 1, 6); }
        }
        if constexpr (u == 29 && i == 2) {
          asm volatile("s_waitcnt vmcnt(0)" : "+v"(st1), "+v"(epv), "+v"(xres[0]), "+v"(xres[1]), "+v"(xres[2]), "+v"(xres[3])::"memory");
        }
        SCHED();
      });
    };
    BAR();
    sfor<0, 7>([&](auto i) { f_rd(fa, 0, i.v); });
    sfor<0, 15>([&](auto k) { p1_step(IC<2 * k.v>{}, fa, fb); p1_step(IC<2 * k.v + 1>{}, fb, fa); });
    tag = (((u64)rfl(epv.y) << 32) | rfl(epv.x)) + 1;
    const u32 tag32 = (u32)tag ^ (u32)(tag >> 32) ^ 0x9E3779B9u;
    for (u32 spins = 0; spins < (1u << 20); ++spins) {
      if (__all(st1.y == tag32 && st1.w == tag32)) break;
      __builtin_amdgcn_s_sleep(1);
      ldsc1<0>(st1, rsW, ST1_OFF + (u32)(g * 4096 + (16 + 32 * w + l32) * 16), 0);
      wait_for<0>(st1);
    }
    const float mean = asf(st1.x), rstd = asf(st1.z);
    LAS float* cs_ = lp<float>(M_CS);
    LAS float* mr = lp<float>(M_MR) + w * 64;
    if (lane < 32) { mr[lane] = mean; mr[32 + lane] = rstd; }
    p1_step(IC<30>{}, fa, fb);
    f32x4 mv[4], rv[4];
#pragma unroll
    for (int gg = 0; gg < 4; ++gg) { mv[gg] = *(LAS f32x4*)(mr + 8 * gg + 4 * hh); rv[gg] = *(LAS f32x4*)(mr + 32 + 8 * gg + 4 * hh); }
    p1_step(IC<31>{}, fb, fa);
    STAMP(1);
    BAR();
    f32x4 cq[4], ck[4];
#pragma unroll
    for (int gg = 0; gg < 4; ++gg) { cq[gg] = *(LAS f32x4*)(cs_ + 8 * gg + 4 * hh); ck[gg] = *(LAS f32x4*)(cs_ + 32 + 8 * gg + 4 * hh); }
    const float csv = cs_[64 + l32];
    SCHED();
    v2u qp_[2][2], kp[2][2], vp[2][2];
    {
      u32 pq[8], pk[8], pv[8];
#pragma unroll
      for (int gg = 0; gg < 4; ++gg) {
        float a[4], k4[4], v4[4];
#pragma unroll
        for (int r = 0; r < 4; ++r) {
          a[r] = __builtin_fmaf(-mean, cq[gg][r], aq[4 * gg + r]);
          k4[r] = rstd * __builtin_fmaf(-mean, ck[gg][r], ak[4 * gg + r]);
          v4[r] = rv[gg][r] * __builtin_fmaf(-mv[gg][r], csv, av[4 * gg + r]);
        }
        pq[2 * gg] = pack2(a[0], a[1]); pq[2 * gg + 1] = pack2(a[2], a[3]);
        pk[2 * gg] = pack2(k4[0], k4[1]); pk[2 * gg + 1] = pack2(k4[2], k4[3]);
        pv[2 * gg] = pack2(v4[0], v4[1]); pv[2 * gg + 1] = pack2(v4[2], v4[3]);
      }
#pragma unroll
      for (int x = 0; x < 2; ++x) {
#pragma unroll
        for (int d = 0; d < 2; ++d) {
          const auto sq_ = __builtin_amdgcn_permlane16_swap(pq[4 * x + d], pq[4 * x + 2 + d], false, false);
          qp_[0][x][d] = sq_[0]; qp_[1][x][d] = sq_[1];
          const auto sk_ = __builtin_amdgcn_permlane16_swap(pk[4 * x + d], pk[4 * x + 2 + d], false, false);
          kp[0][x][d] = sk_[0]; kp[1][x][d] = sk_[1];
          const auto s1 = __builtin_amdgcn_permlane32_swap(pv[4 * x + d], pv[4 * x + 2 + d], false, false);
          const auto s2 = __builtin_amdgcn_permlane16_swap(s1[0], s1[1], false, false);
          vp[x][0][d] = s2[0]; vp[x][1][d] = s2[1];
        }
      }
    }
    {
#pragma unroll
      for (int hd = 0; hd < 2; ++hd) {
        *lp<v2u>(KV_OFF + w * 2048 + hd * 512 + lane * 8) = kp[1][hd];
        *lp<v2u>(KV_OFF + w * 2048 + (2 + hd) * 512 + lane * 8) = vp[1][hd];
      }
    }
    WAITL0();
    BAR();
    v2u kprev[2], vprev[2];
    {
      const int pb = w == 0 ? KVH_OFF : KV_OFF + (w - 1) * 2048;
#pragma unroll
      for (int hd = 0; hd < 2; ++hd) {
        kprev[hd] = *lp<v2u>(pb + hd * 512 + lane * 8);
        vprev[hd] = *lp<v2u>(pb + (2 + hd) * 512 + lane * 8);
      }
    }
    const auto rq = __builtin_amdgcn_permlane16_swap(asu(rstd), asu(rstd), false, false);
    const v2u ones2 = {0x3F803F80u, 0x3F803F80u};
    auto attend = [&](int t, int hd, v2u kA, v2u vA, bool prev_ok) {
      const float sc = SC * asf(rq[t]);
      f32x4 s0 = mfma16k16(kA, qp_[t][hd], f32x4{});
      f32x4 s1 = mfma16k16(kp[t][hd], qp_[t][hd], f32x4{});
      float e[8];
#pragma unroll
      for (int r = 0; r < 4; ++r) {
        e[r] = __builtin_fmaf(s0[r], sc, prev_ok ? bias0[r] : NEG);
        e[4 + r] = __builtin_fmaf(s1[r], sc, bias1[r]);
      }
      float mx = fmaxf(fmaxf(fmaxf(e[0], e[1]), fmaxf(e[2], e[3])), fmaxf(fmaxf(e[4], e[5]), fmaxf(e[6], e[7])));
      mx = xmax32(xmax16(mx));
#pragma unroll
      for (int r = 0; r < 8; ++r) e[r] = __builtin_amdgcn_exp2f(e[r] - mx);
      const v2u p0 = {pack2(e[0], e[1]), pack2(e[2], e[3])}, p1 = {pack2(e[4], e[5]), pack2(e[6], e[7])};
      f32x4 sm = mfma16k16(ones2, p0, f32x4{});
      sm = mfma16k16(ones2, p1, sm);
      f32x4 o = mfma16k16(vA, p0, f32x4{});
      o = mfma16k16(vp[t][hd], p1, o);
      const float inv = __builtin_amdgcn_rcpf(sm[0]);
      return v2u{pack2(o[0] * inv, o[1] * inv), pack2(o[2] * inv, o[3] * inv)};
    };
    {
      const bool ok = T0 + 32 * w > 0;
      const v2u o10 = attend(1, 0, kp[0][0], vp[0][0], true), o11 = attend(1, 1, kp[0][1], vp[0][1], true);
      const v2u o00 = attend(0, 0, kprev[0], vprev[0], ok), o01 = attend(0, 1, kprev[1], vprev[1], ok);
      stsc1(rsW, O_OFF + (u32)(((g * 8 + 2 * w + 1) * 16 + m) * 1024 + lane * 16), v4u{o10.x, o10.y, o11.x, o11.y});
      stsc1(rsW, O_OFF + (u32)(((g * 8 + 2 * w) * 16 + m) * 1024 + lane * 16), v4u{o00.x, o00.y, o01.x, o01.y});
    }
  }
  STAMP(2);
  WAITVN(0);
  publish(fl, m, tag);
  STAMP(3);

  {
    bf16x8 w2[8][4];
    int a[4];
    {
      const int row = 4 * q + qp, f = ((row >> 1) & 3) << 1;
#pragma unroll
      for (int n = 0; n < 4; ++n) a[n] = opq(W2_OFF + row * 128 + (((2 * n + (pp >> 1)) ^ f) * 16) + (pp & 1) * 8);
#pragma unroll
      for (int s = 0; s < 8; ++s)
#pragma unroll
        for (int n = 0; n < 4; ++n) w2[s][n] = tr2(a[n] + s * 4096, 2048);
    }
    poll(fl, m, 16, tag);
    BAR();
    STAMP(4);
    v4u of[16];
    const u32 ob = O_OFF + (u32)((g * 8 + 4 * th + w) * 16) * 1024;
    sfor<0, 16>([&](auto i) { ldsc1<0>(of[i.v], rsW, lane * 16, ob + (u32)((i.v + 2 * ct) & 15) * 1024); });
    f32x4 acc[4] = {};
    sfor<0, 16>([&](auto sc) {
      constexpr int s = sc.v;
      wait_for<15 - s>(of[s]);
      const bf16x8 bfr = __builtin_bit_cast(bf16x8, of[s]);
#pragma unroll
      for (int n = 0; n < 4; ++n) {
        acc[n] = mfma16(w2[s & 7][n], bfr, acc[n]);
        if constexpr (s < 8) w2[s][n] = tr2(a[n] + (s + 8) * 4096, 2048);
      }
      SCHED();
    });
    STAMP(5);
    float s = 0.f, ss = 0.f;
    const int tl = 16 * w + tok;
    const int t32 = g * 4 + 2 * th + (w >> 1);
    const u32 hoff = H_OFF + (u32)((t32 * 32 + 4 * ct) * 1024 + (16 * (w & 1) + tok + 32 * (q >> 1)) * 16 + (q & 1) * 8);
#pragma unroll
    for (int n = 0; n < 4; ++n) {
      const u32 h01 = pack2(bflo(xres[n].x) + acc[n][0], bfhi(xres[n].x) + acc[n][1]);
      const u32 h23 = pack2(bflo(xres[n].y) + acc[n][2], bfhi(xres[n].y) + acc[n][3]);
      const v2u hp = {h01, h23};
      stsc1_8(rsW, hoff + n * 1024, hp);
      *lp<v2u>(M_HK + tl * HK_PITCH + (16 * n + 4 * q) * 2) = hp;
      const float a0 = bflo(h01), a1 = bfhi(h01), a2 = bflo(h23), a3 = bfhi(h23);
      s += (a0 + a1) + (a2 + a3);
      ss = __builtin_fmaf(a0, a0, ss); ss = __builtin_fmaf(a1, a1, ss); ss = __builtin_fmaf(a2, a2, ss); ss = __builtin_fmaf(a3, a3, ss);
    }
    s = xadd32(xadd16(s)); ss = xadd32(xadd16(ss));
    if (q == 0) stsc1_8(rsW, ST_OFF + (u32)((ct * S + T0 + 64 * th + tl) * 8), v2u{asu(s), asu(ss)});
  }
  WAITVN(0);
  WAITL0();
  publish(fl + 256, m, tag);
  STAMP(6);

  {
    const int wa = (8 * hh + qp) * 192 + (q & 1) * 32 + pp * 8;
    bf16x8 wA[6], wB[6];
    auto w3_read1 = [&](bf16x8& dst, int base, int i) { const int o = base + (i & 1) * 12288 + (i >> 1) * 64; dst = tr2(o, 768); };
    poll(fl + 256, m, 16, tag);
    BAR();
    STAMP(7);
    const int t32 = g * 4 + w;
    v2u st[4];
#pragma unroll
    for (int i = 0; i < 4; ++i) ldsc1_8(st[i], rsW, (u32)(((hh * 4 + i) * S + l32) * 8), ST_OFF + (u32)(t32 * 32) * 8);
    v4u hf[16];
    const u32 hb = H_OFF + (u32)(t32 * 32) * 1024;
    auto ldh = [&](auto i) { ldsc1<(i.v & 3) * 1024>(hf[i.v & 15], rsW, lane * 16, hb + (u32)(((i.v >> 2) + m) & 7) * 4096); };
    sfor<0, 16>(ldh);
#pragma unroll
    for (int i = 0; i < 6; ++i) w3_read1(wA[i], wa, i);
    asm volatile("s_waitcnt vmcnt(16)" : "+v"(st[0]), "+v"(st[1]), "+v"(st[2]), "+v"(st[3])::"memory");
    float rs, nb;
    {
      float s = (asf(st[0].x) + asf(st[1].x)) + (asf(st[2].x) + asf(st[3].x)), ss = (asf(st[0].y) + asf(st[1].y)) + (asf(st[2].y) + asf(st[3].y));
      s = xadd32(s); ss = xadd32(ss);
      const float mu = s * (1.0f / D);
      rs = __builtin_amdgcn_rsqf(fmaxf(__builtin_fmaf(-mu, mu, ss * (1.0f / D)), 0.f) + 1e-5f);
      nb = -mu * rs;
    }
    auto ln = [&](v4u h, int d) {
      const f32x2 v = {bflo(h[d]), bfhi(h[d])};
      const f32x2 r = __builtin_elementwise_fma(v, f32x2{rs, rs}, f32x2{nb, nb});
      return pack2(r[0], r[1]);
    };
    v4u yA, yB;
    wait_for<15>(hf[0]);
#pragma unroll
    for (int d = 0; d < 4; ++d) yA[d] = ln(hf[0], d);
    ldh(IC<16>{});
    f32x16 acc[6] = {};
    auto step = [&](auto sc, bf16x8(&wc)[6], bf16x8(&wn)[6], v4u& yc, v4u& yn) {
      constexpr int s = sc.v, c1 = (s + 1) >> 2;
      if constexpr (s + 1 < 32) wait_for<(16 + s < 31 ? 16 + s : 31) - (s + 1)>(hf[(s + 1) & 15]);
      if constexpr (s >= 4 && (s & 3) == 0) BAR();
      const int base = opq(wa + (c1 % 6) * SLOT + (16 * ((s + 1) & 3)) * 192);
      const bf16x8 yb = __builtin_bit_cast(bf16x8, yc);
      __builtin_amdgcn_s_waitcnt(0xC07F);
      sfor<0, 6>([&](auto ic) {
        constexpr int i = ic.v;
        acc[i] = mfma32(wc[i], yb, acc[i]);
        if constexpr (s + 1 < 32) {
          if constexpr (i < 3) { w3_read1(wn[2 * i], base, 2 * i); w3_read1(wn[2 * i + 1], base, 2 * i + 1); }
          if constexpr (i >= 2) yn[i - 2] = ln(hf[(s + 1) & 15], i - 2);
        }
        if constexpr (i == 5 && 17 + s < 32) ldh(IC<(17 + s) & 31>{});
        SCHED();
      });
    };
    sfor<0, 16>([&](auto k) { step(IC<2 * k.v>{}, wA, wB, yA, yB); step(IC<2 * k.v + 1>{}, wB, wA, yB, yA); });
    STAMP(8);
    BAR();
#pragma unroll
    for (int i = 0; i < 3; ++i) {
      const int f0 = 96 * m + 32 * i;
      if (f0 < F) {
#pragma unroll
        for (int s = 0; s < 2; ++s) {
          v4u o;
#pragma unroll
          for (int d = 0; d < 4; ++d) {
            const int r = 8 * s + 2 * d;
            o[d] = pack2(fmaxf(acc[2 * i][r], 0.f) * acc[2 * i + 1][r], fmaxf(acc[2 * i][r + 1], 0.f) * acc[2 * i + 1][r + 1]);
          }
          stsc1(rsW, A_OFF + (u32)((t32 * 86 + f0 / 16 + s) * 1024 + lane * 16), o);
        }
      }
    }
    WAITVN(0);
  }
  publish(fl + 512, m, tag);
  STAMP(9);

  {
    poll(fl + 512, m, 15, tag);
    BAR();
    STAMP(10);
    const int mh = w & 1, kh = w >> 1;
    const int t32 = g * 4 + 2 * th + mh;
    v4u af[24];
    const u32 ab = A_OFF + (u32)(t32 * 86 + kh * 43) * 1024;
    auto lda = [&](auto i) { const int ps = i.v + 5 * ct; ldsc1<0>(af[i.v % 24], rsW, lane * 16, ab + (u32)(ps >= 43 ? ps - 43 : ps) * 1024); };
    sfor<0, 24>(lda);
    f32x16 acc[2] = {};
    int wa[2];
#pragma unroll
    for (int n = 0; n < 2; ++n) wa[n] = (4 * hh + qp) * 128 + ((n ^ ((qp >> 1) & 1)) * 64) + (q & 1) * 32 + pp * 8;
    bf16x8 wA[2], wB[2];
    auto w4_read = [&](bf16x8(&dst)[2], auto sc) {
      constexpr int s = sc.v;
      const int off = s < 42 ? ((s / 6 + 2) % 6) * SLOT + kh * 12288 + (s % 6) * 2048 : M_W4T + kh * 2048;
#pragma unroll
      for (int n = 0; n < 2; ++n) dst[n] = tr2(opq(wa[n] + off), 1024);
    };
    w4_read(wA, IC<0>{});
    auto step = [&](auto sc, bf16x8(&wc)[2], bf16x8(&wn)[2]) {
      constexpr int s = sc.v;
      wait_for<(23 + s < 42 ? 23 + s : 42) - s>(af[s % 24]);
      if constexpr (s == 6 || s == 30) BAR();
      if constexpr (s + 1 < 43) w4_read(wn, IC<s + 1>{});
      const bf16x8 bfr = __builtin_bit_cast(bf16x8, af[s % 24]);
      acc[0] = mfma32(wc[0], bfr, acc[0]);
      acc[1] = mfma32(wc[1], bfr, acc[1]);
      if constexpr (24 + s < 43) lda(IC<(24 + s < 43 ? 24 + s : 0)>{});
      SCHED();
    };
    sfor<0, 21>([&](auto k) { step(IC<2 * k.v>{}, wA, wB); step(IC<2 * k.v + 1>{}, wB, wA); });
    step(IC<42>{}, wA, wB);
    STAMP(11);
    LAS f32x4* red = lp<f32x4>(3 * SLOT);
    {
      const f32x16 snd = kh ? acc[0] : acc[1];
#pragma unroll
      for (int gg = 0; gg < 4; ++gg) red[(w * 4 + gg) * 64 + lane] = f32x4{snd[4 * gg], snd[4 * gg + 1], snd[4 * gg + 2], snd[4 * gg + 3]};
    }
    if (m == 0 && tid == 0) __hip_atomic_store(ep, tag, RLX_AGENT);
    WAITL0(); BAR();
    {
      const f32x16 keep = kh ? acc[1] : acc[0];
      const int pw = w ^ 2;
      GAS u16* orow = (GAS u16*)p.out + (size_t)(T0 + 64 * th + 32 * mh + l32) * D + 64 * ct + 32 * kh + 4 * hh;
#pragma unroll
      for (int gg = 0; gg < 4; ++gg) {
        const f32x4 o = red[(pw * 4 + gg) * 64 + lane];
        const v2u hk = *lp<v2u>(M_HK + (32 * mh + l32) * HK_PITCH + (32 * kh + 8 * gg + 4 * hh) * 2);
        v2u r;
        r.x = pack2(bflo(hk.x) + (keep[4 * gg] + o[0]), bfhi(hk.x) + (keep[4 * gg + 1] + o[1]));
        r.y = pack2(bflo(hk.y) + (keep[4 * gg + 2] + o[2]), bfhi(hk.y) + (keep[4 * gg + 3] + o[3]));
        *(GAS v2u*)(orow + 8 * gg) = r;
      }
    }
  }
  STAMP(12);
}

extern "C" void kernel_launch(void* const* d_in, const int* in_sizes, int n_in, void* d_out, int out_size,
                              void* d_ws, size_t ws_size, hipStream_t stream) {
  Params p;
  p.x = (const u16*)d_in[0]; p.wqkv = (const u16*)d_in[1]; p.wo = (const u16*)d_in[2];
  p.wg = (const u16*)d_in[4]; p.wu = (const u16*)d_in[5]; p.wd = (const u16*)d_in[6];
  p.out = (u16*)d_out; p.ws = (unsigned char*)d_ws;
  (void)hipFuncSetAttribute((const void*)fused_block, hipFuncAttributeMaxDynamicSharedMemorySize, LDS_BYTES);
  fused_block<<<256, 512, LDS_BYTES, stream>>>(p);
}
```

```cpp
#include <hip/hip_runtime.h>
#include <stdint.h>

typedef unsigned short u16;
typedef unsigned u32;
typedef unsigned long long u64;
typedef short bf16x8 __attribute__((ext_vector_type(8)));
typedef short bf16x4 __attribute__((ext_vector_type(4)));
typedef float f32x2 __attribute__((ext_vector_type(2)));
typedef float f32x4 __attribute__((ext_vector_type(4)));
typedef float f32x16 __attribute__((ext_vector_type(16)));
typedef unsigned v4u __attribute__((ext_vector_type(4)));
typedef unsigned v2u __attribute__((ext_vector_type(2)));
typedef __bf16 bf2_t __attribute__((ext_vector_type(2)));
#define GAS __attribute__((address_space(1)))
#define LAS __attribute__((address_space(3)))
#define DI __device__ __forceinline__
#define RLX_AGENT __ATOMIC_RELAXED, __HIP_MEMORY_SCOPE_AGENT
typedef GAS u64 gu64;
typedef __amdgpu_buffer_rsrc_t rsrc_t;

constexpr int S = 2048, D = 512, F = 1376;
constexpr u32 O_OFF = 0;
constexpr u32 H_OFF = 2u << 20;
constexpr u32 A_OFF = 4u << 20;
constexpr u32 ST_OFF = 10u << 20;
constexpr u32 ST1_OFF = (10u << 20) + (512u << 10);
constexpr u32 FL_OFF = 11u << 20;
constexpr u32 EP_OFF = 12u << 20;
constexpr u32 WS_BYTES = 13u << 20;

constexpr int SLOT = 24576;
constexpr int MISC = 6 * SLOT;
constexpr int LDS_BYTES = 163840;
constexpr int P1_STAGE = 30720;
constexpr int P1_WOFF = 18432;
constexpr int W2_OFF = 3 * P1_STAGE;
constexpr int M_CS = 157696;
constexpr int M_MR = M_CS + 1024;
constexpr int KVH_OFF = M_CS + 2048;
constexpr int KV_OFF = 3 * SLOT;
constexpr int M_HK = MISC;
constexpr int M_W4T = MISC + 8704;
constexpr int HK_PITCH = 136;

struct Params {
  const u16* x; const u16* wqkv; const u16* wo; const u16* wg; const u16* wu; const u16* wd;
  u16* out; unsigned char* ws;
};

template <int I> struct IC { static constexpr int v = I; };
template <int I, int N, class Fn> DI void sfor(Fn&& f) { if constexpr (I < N) { f(IC<I>{}); sfor<I + 1, N>(f); } }

DI u32 pack2(float a, float b) { f32x2 v = {a, b}; bf2_t r = __builtin_convertvector(v, bf2_t); return __builtin_bit_cast(u32, r); }
DI float bflo(u32 u) { return __builtin_bit_cast(float, u << 16); }
DI float bfhi(u32 u) { return __builtin_bit_cast(float, u & 0xffff0000u); }
DI float asf(u32 u) { return __builtin_bit_cast(float, u); }
DI u32 asu(float f) { return __builtin_bit_cast(u32, f); }
DI f32x4 mfma16(bf16x8 a, bf16x8 b, f32x4 c) { return __builtin_amdgcn_mfma_f32_16x16x32_bf16(a, b, c, 0, 0, 0); }
DI f32x4 mfma16k16(v2u a, v2u b, f32x4 c) { return __builtin_amdgcn_mfma_f32_16x16x16bf16_1k(__builtin_bit_cast(bf16x4, a), __builtin_bit_cast(bf16x4, b), c, 0, 0, 0); }
DI f32x16 mfma32(bf16x8 a, bf16x8 b, f32x16 c) { return __builtin_amdgcn_mfma_f32_32x32x16_bf16(a, b, c, 0, 0, 0); }
DI bf16x8 cat(bf16x4 a, bf16x4 b) { return __builtin_shufflevector(a, b, 0, 1, 2, 3, 4, 5, 6, 7); }
#pragma clang diagnostic ignored "-Wint-to-pointer-cast"
DI bf16x4 trr(int off) { return __builtin_amdgcn_ds_read_tr16_b64_v4i16((LAS bf16x4*)(u32)off); }
DI bf16x8 tr2(int off, int d) { return cat(trr(off), trr(off + d)); }
DI bf16x8 ldsb(int off) { return *(LAS bf16x8*)(u32)off; }
template <class T> DI LAS T* lp(int off) { return (LAS T*)(u32)off; }
DI rsrc_t mkrs(const void* p, u32 bytes) { return __builtin_amdgcn_make_buffer_rsrc((void*)p, 0, bytes, 0x00020000); }
DI u32 rfl(u32 v) { return __builtin_amdgcn_readfirstlane(v); }
DI int opq(int v) { asm volatile("" : "+v"(v)); return v; }
DI float xmax16(float v) { auto r = __builtin_amdgcn_permlane16_swap(asu(v), asu(v), false, false); return fmaxf(asf(r[0]), asf(r[1])); }
DI float xmax32(float v) { auto r = __builtin_amdgcn_permlane32_swap(asu(v), asu(v), false, false); return fmaxf(asf(r[0]), asf(r[1])); }
DI float xadd16(float v) { auto r = __builtin_amdgcn_permlane16_swap(asu(v), asu(v), false, false); return asf(r[0]) + asf(r[1]); }
DI float xadd32(float v) { auto r = __builtin_amdgcn_permlane32_swap(asu(v), asu(v), false, false); return asf(r[0]) + asf(r[1]); }
#define BAR() __builtin_amdgcn_s_barrier()
#define SCHED() __builtin_amdgcn_sched_barrier(0)
#define WAITVN(n) asm volatile("s_waitcnt vmcnt(%0)" ::"i"(n) : "memory")
#define WAITL0() asm volatile("s_waitcnt lgkmcnt(0)" ::: "memory")

DI void dma(rsrc_t rs, u32 voff, u32 soff, u32 ldsaddr) {
  asm volatile("s_mov_b32 m0, %0\n\ts_nop 0\n\tbuffer_load_dwordx4 %1, %2, %3 offen lds" ::"s"(ldsaddr), "v"(voff), "s"(rs), "s"(soff) : "memory");
}
template <int IMM> DI void ldsc1(v4u& dst, rsrc_t rs, u32 voff, u32 soff) {
  asm volatile("buffer_load_dwordx4 %0, %1, %2, %3 offen offset:%4 sc1" : "=v"(dst) : "v"(voff), "s"(rs), "s"(soff), "i"(IMM) : "memory");
}
DI void ldsc1_8(v2u& dst, rsrc_t rs, u32 voff, u32 soff) {
  asm volatile("buffer_load_dwordx2 %0, %1, %2, %3 offen sc1" : "=v"(dst) : "v"(voff), "s"(rs), "s"(soff) : "memory");
}
DI void ld16(v4u& dst, rsrc_t rs, u32 voff, u32 soff) {
  asm volatile("buffer_load_dwordx4 %0, %1, %2, %3 offen" : "=v"(dst) : "v"(voff), "s"(rs), "s"(soff) : "memory");
}
DI void ld4_keep(u32& dst, rsrc_t rs, u32 voff) { asm volatile("buffer_load_dword %0, %1, %2, 0 offen" : "+v"(dst) : "v"(voff), "s"(rs) : "memory"); }
template <int IMM> DI void ld8(v2u& dst, rsrc_t rs, u32 voff, u32 soff) {
  asm volatile("buffer_load_dwordx2 %0, %1, %2, %3 offen offset:%4" : "=v"(dst) : "v"(voff), "s"(rs), "s"(soff), "i"(IMM) : "memory");
}
template <int N> DI void wait_for(v4u& a) { asm volatile("s_waitcnt vmcnt(%1)" : "+v"(a) : "i"(N > 63 ? 63 : N) : "memory"); }
DI void stsc1(rsrc_t rs, u32 off, v4u v) { __builtin_amdgcn_raw_buffer_store_b128(v, rs, off, 0, 16); }
DI void stsc1_8(rsrc_t rs, u32 off, v2u v) { __builtin_amdgcn_raw_buffer_store_b64(v, rs, off, 0, 16); }

#define STAMP(i) do {} while (0)

DI void publish(gu64* fl, int m, u64 tag) {
  BAR();
  if (threadIdx.x < 16) __hip_atomic_store(fl + threadIdx.x * 16 + m, tag, RLX_AGENT);
}
DI void poll(gu64* fl, int m, int nprod, u64 tag) {
  if (threadIdx.x < 64) {
    const int lane = threadIdx.x;
    for (u32 spins = 0; spins < (1u << 20); ++spins) {
      u64 v = tag;
      if (lane < nprod) v = __hip_atomic_load(fl + m * 16 + lane, RLX_AGENT);
      if (__all(v == tag)) break;
    }
    __builtin_amdgcn_fence(__ATOMIC_ACQUIRE, "wavefront");
  }
}

__global__ __launch_bounds__(512, 2) void fused_block(Params p) {
  extern __shared__ __attribute__((aligned(16))) unsigned char smem[];
  const int tid = threadIdx.x, lane = tid & 63, w8 = (int)rfl(tid >> 6), w = w8 & 3;
  const int b = blockIdx.x, g = (b & 7) * 2 + ((b >> 3) >> 4), m = (b >> 3) & 15;
  const int T0 = g * 128;
  const int tok = lane & 15, q = lane >> 4;
  const int th = m >> 3, ct = m & 7;
  const rsrc_t rsW = mkrs(p.ws, WS_BYTES);
  const rsrc_t rsX = mkrs(p.x, S * D * 2);
  const u32 halo0 = (u32)(T0 >= 16 ? T0 - 16 : 0);
  asm volatile("" ::"s"((u32)(size_t)smem));

  if (w8 >= 4) {
    const rsrc_t rsW1 = mkrs(p.wqkv, D * 3 * D * 2);
    v2u epv;
    ldsc1_8(epv, rsW, 0, EP_OFF + g * 128);
    u32 vx0, vx1, vxh, vw0, vw1, vw2;
    {
      const int r8 = lane >> 3, sl = lane & 7;
      vx0 = r8 * 1024 + ((sl ^ ((lane >> 4) & 7)) * 16);
      vx1 = r8 * 1024 + ((sl ^ ((4 + (lane >> 4)) & 7)) * 16);
      vxh = (w & 1) ? vx1 : vx0;
      auto pat = [&](int j) { const int e = j * 64 + lane, r = e / 12, pc = e % 12; return (u32)(r * 3072 + (pc >> 2) * 1024 + (pc & 3) * 16); };
      vw0 = pat(0); vw1 = pat(1); vw2 = pat(2);
    }
    const u32 sxh = (halo0 + 8 * (w & 1)) * 1024, sxm = (u32)(T0 + 32 * w) * 1024, sw1 = (u32)(16 * w) * 3072 + m * 64;
    const int rot = (g + m) & 7;
    auto p1_piece = [&](int cc, int j) {
      const u32 lb = (cc % 5) * P1_STAGE;
      const u32 c = (u32)((cc + rot) & 7);
      if (j == 0) dma(rsX, vxh, sxh + c * 128, lb + (w & 1) * 1024);
      if (j == 1) dma(rsW1, vw0, sw1 + c * (64 * 3072), lb + P1_WOFF + (3 * w) * 1024);
      if (j == 2) dma(rsX, vx0, sxm + c * 128, lb + (2 + 4 * w) * 1024);
      if (j == 3) dma(rsW1, vw1, sw1 + c * (64 * 3072), lb + P1_WOFF + (3 * w + 1) * 1024);
      if (j == 4) dma(rsX, vx1, sxm + c * 128 + 8192, lb + (3 + 4 * w) * 1024);
      if (j == 5) dma(rsW1, vw2, sw1 + c * (64 * 3072), lb + P1_WOFF + (3 * w + 2) * 1024);
      if (j == 6) dma(rsX, vx0, sxm + c * 128 + 16384, lb + (4 + 4 * w) * 1024);
      if (j == 7) dma(rsX, vx1, sxm + c * 128 + 24576, lb + (5 + 4 * w) * 1024);
    };
    const rsrc_t rsW2 = mkrs(p.wo, D * D * 2), rsG = mkrs(p.wg, D * F * 2), rsU = mkrs(p.wu, D * F * 2), rsD = mkrs(p.wd, F * D * 2);
    const u32 v2 = (lane >> 3) * 1024 + (((lane & 7) ^ (((lane >> 4) & 3) << 1)) * 16);
    auto w2_piece = [&](int i) {
      const int ii = 16 * w + i, ls = ii >> 2;
      dma(rsW2, v2, (u32)(32 * ((ls + 2 * ct) & 15) + 8 * (ii & 3)) * 1024 + ct * 128, W2_OFF + ii * 1024);
    };
    const int qp = (lane & 15) >> 2, pp = lane & 3;
    f32x4 ahA = {}, ahB = {};
    const int xha0 = tok * 128 + ((q ^ (tok >> 1)) * 16), xha1 = tok * 128 + (((4 + q) ^ (tok >> 1)) * 16);
    const int wha = P1_WOFF + (8 * q + qp) * 192 + (1 + (w >> 1)) * 64 + (w & 1) * 32 + pp * 8;
    const int whb = P1_WOFF + (8 * q + qp) * 192 + (w < 2 ? 0 : 128) + (w & 1) * 32 + pp * 8;
    auto halo_with = [&](int hc, auto&& issue) {
      const int sb = (hc % 5) * P1_STAGE;
      v4u xh[2]; bf16x8 fa[2], fb[2];
#pragma unroll
      for (int s2 = 0; s2 < 2; ++s2) {
        xh[s2] = __builtin_bit_cast(v4u, ldsb((s2 ? xha1 : xha0) + sb));
        fa[s2] = tr2(wha + sb + s2 * 6144, 768); fb[s2] = tr2(whb + sb + s2 * 6144, 768);
      }
      SCHED();
      issue(0); issue(1); issue(2); issue(3);
      SCHED();
#pragma unroll
      for (int s2 = 0; s2 < 2; ++s2) {
#pragma unroll
        for (int d = 0; d < 4; ++d) xh[s2][d] = tok == 0 ? 0x3F803F80u : xh[s2][d];
        const bf16x8 xhb = __builtin_bit_cast(bf16x8, xh[s2]);
        ahA = mfma16(fa[s2], xhb, ahA);
        ahB = mfma16(fb[s2], xhb, ahB);
      }
      SCHED();
      issue(4); issue(5); issue(6); issue(7);
    };
    sfor<0, 24>([&](auto i) { p1_piece(i.v >> 3, i.v & 7); });
    WAITVN(16); BAR();
    halo_with(0, [&](int j) { p1_piece(3, j); });
    WAITVN(16); BAR();
    halo_with(1, [&](int j) { p1_piece(4, j); });
    WAITVN(16); BAR();
    halo_with(2, [&](int j) { p1_piece(5, j); });
    WAITVN(16); BAR();
    halo_with(3, [&](int j) { p1_piece(6, j); });
    WAITVN(16); BAR();
    halo_with(4, [&](int j) { p1_piece(7, j); });
    u32 touch = 0;
    {
      const u32 wg_ = (u32)(b * 4 + w);
      const u32 o4 = (u32)lane < 4u ? (wg_ * 4u + lane) * 128u : 0xFFFFFF00u, o11 = (u32)lane < 11u ? (wg_ * 11u + lane) * 128u : 0xFFFFFF00u;
      ld4_keep(touch, rsW2, o4); ld4_keep(touch, rsG, o11); ld4_keep(touch, rsU, o11); ld4_keep(touch, rsD, o11);
    }
    v4u sth;
    ldsc1<0>(sth, rsW, ST1_OFF + (u32)(g * 4096 + tok * 16), 0);
    WAITVN(21); BAR();
    halo_with(5, [&](int) {});
    WAITVN(13); BAR();
    halo_with(6, [&](int) {});
    WAITVN(5); BAR();
    halo_with(7, [&](int j) { w2_piece(j); });
    asm volatile("s_waitcnt vmcnt(8)" : "+v"(sth), "+v"(touch), "+v"(epv)::"memory");
    const u64 tag = (((u64)rfl(epv.y) << 32) | rfl(epv.x)) + 1;
    const u32 tag32 = (u32)tag ^ (u32)(tag >> 32) ^ 0x9E3779B9u;
    for (u32 spins = 0; spins < (1u << 20); ++spins) {
      if (__all(sth.y == tag32 && sth.w == tag32)) break;
      __builtin_amdgcn_s_sleep(1);
      ldsc1<0>(sth, rsW, ST1_OFF + (u32)(g * 4096 + tok * 16), 0);
      wait_for<0>(sth);
    }
    {
      LAS float* cs_ = lp<float>(M_CS);
      if (tok == 0) {
        *(LAS f32x4*)(cs_ + 32 + 16 * w + 4 * q) = ahA;
        if (w < 2) *(LAS f32x4*)(cs_ + 16 * w + 4 * q) = ahB;
      }
      const float hmu = asf(sth.x), hrs = asf(sth.z);
      float hv[4];
#pragma unroll
      for (int r = 0; r < 4; ++r) {
        const float c = asf(__builtin_amdgcn_update_dpp(0, asu(ahA[r]), 0x150, 0xf, 0xf, false));
        hv[r] = hrs * __builtin_fmaf(-hmu, c, ahA[r]);
      }
      const u32 o0 = pack2(hv[0], hv[1]), o1 = pack2(hv[2], hv[3]);
      if (w < 2) *lp<v2u>(KVH_OFF + w * 512 + (tok + 16 * (((q & 1) << 1) | (q >> 1))) * 8) = v2u{o0, o1};
      else {
        LAS u16* vv = lp<u16>(KVH_OFF + w * 512) + (4 * q + 16 * (tok >> 2)) * 4 + (tok & 3);
        vv[0] = (u16)o0; vv[4] = (u16)(o0 >> 16); vv[8] = (u16)o1; vv[12] = (u16)(o1 >> 16);
      }
    }
    WAITL0();
    const u32 v30 = (lane / 12) * (F * 2) + (lane % 12) * 16, v31 = ((64 + lane) / 12) * (F * 2) + ((64 + lane) % 12) * 16,
              v32 = ((128 + lane) / 12) * (F * 2) + ((128 + lane) % 12) * 16;
    const u32 s3 = (u32)(16 * w) * (F * 2) + m * 192;
    auto w3_chunk = [&](int c) {
      const u32 so = s3 + (u32)(64 * ((c + m) & 7)) * (F * 2), lb = (c % 6) * SLOT + 3 * w * 1024;
      dma(rsG, v30, so, lb); dma(rsG, v31, so, lb + 1024); dma(rsG, v32, so, lb + 2048);
      dma(rsU, v30, so, lb + 12288); dma(rsU, v31, so, lb + 12288 + 1024); dma(rsU, v32, so, lb + 12288 + 2048);
    };
    const u32 v4s = (lane >> 3) * 1024 + (((lane & 7) ^ (((lane >> 4) & 1) << 2)) * 16);
    auto w4_row = [&](int ls) { const int ps = ls + 5 * ct; return (u32)((ps >= 43 ? ps - 43 : ps) * 16); };
    auto w4_part = [&](int part) {
      const u32 lb = ((part + 2) % 6) * SLOT + 3 * w * 1024;
#pragma unroll
      for (int j = 0; j < 3; ++j) {
        const int jj = 3 * w + j;
        const u32 so = (w4_row(6 * part + (jj >> 1)) + (jj & 1) * 8) * 1024 + ct * 128;
        dma(rsD, v4s, so, lb + j * 1024); dma(rsD, v4s, so + 688 * 1024, lb + 12288 + j * 1024);
      }
    };
    BAR();
    sfor<8, 16>([&](auto i) { w2_piece(i.v); });
    BAR();
    w3_chunk(0); w3_chunk(1);
    WAITVN(12);
    BAR();
    w3_chunk(2);
    BAR();
    BAR();
    w3_chunk(3);
    WAITVN(6); BAR();
    w3_chunk(4); w3_chunk(5);
    BAR(); w3_chunk(6);
    WAITVN(18); BAR(); w3_chunk(7);
    WAITVN(18); BAR(); w4_part(0);
    WAITVN(18); BAR(); w4_part(1);
    WAITVN(18); BAR(); w4_part(2);
    WAITVN(18); BAR(); w4_part(3);
    BAR(); w4_part(4);
    BAR();
    w4_part(5);
    dma(rsD, v4s, ((u32)(w >> 1) * 688 + w4_row(42) + (w & 1) * 8) * 1024 + ct * 128, M_W4T + w * 1024);
    BAR();
    WAITVN(7); BAR();
    WAITVN(0); BAR(); w4_part(6);
    WAITVN(0); BAR();
    BAR();
    return;
  }

  STAMP(0);
  gu64* ep = (gu64*)(p.ws + EP_OFF) + g * 16;
  gu64* fl = (gu64*)(p.ws + FL_OFF) + g * 768;
  v2u epv, xres[4];
  v4u srow[3];
  ldsc1_8(epv, rsW, 0, EP_OFF + g * 128);
  sfor<0, 3>([&](auto jc) {
    const int j = jc.v < 2 ? w + 4 * jc.v : 8, R = 9 * m + j;
    const u32 token = R < 16 ? halo0 + R : (u32)(T0 + R - 16);
    ld16(srow[jc.v], rsX, lane * 16, token * 1024);
  });
  {
    const u32 vo = (u32)tok * 1024 + q * 8, so = (u32)(T0 + 64 * th + 16 * w) * 1024 + ct * 128;
    ld8<0>(xres[0], rsX, vo, so); ld8<32>(xres[1], rsX, vo, so); ld8<64>(xres[2], rsX, vo, so); ld8<96>(xres[3], rsX, vo, so);
  }
  asm volatile("s_waitcnt vmcnt(4)" : "+v"(epv), "+v"(srow[0]), "+v"(srow[1]), "+v"(srow[2])::"memory");
  const u64 tag = (((u64)rfl(epv.y) << 32) | rfl(epv.x)) + 1;
  const u32 tag32 = (u32)tag ^ (u32)(tag >> 32) ^ 0x9E3779B9u;
    {
    float sv[3], qv[3];
#pragma unroll
    for (int j = 0; j < 3; ++j) {
      float s = 0.f, ss = 0.f;
#pragma unroll
      for (int d = 0; d < 4; ++d) {
        const float a = bflo(srow[j][d]), c = bfhi(srow[j][d]);
        s += a + c; ss = __builtin_fmaf(a, a, ss); ss = __builtin_fmaf(c, c, ss);
      }
      sv[j] = s; qv[j] = ss;
    }
    auto s32 = [&](float a, float c) { auto r = __builtin_amdgcn_permlane32_swap(asu(a), asu(c), false, false); return asf(r[0]) + asf(r[1]); };
    auto s16 = [&](float a, float c) { auto r = __builtin_amdgcn_permlane16_swap(asu(a), asu(c), false, false); return asf(r[0]) + asf(r[1]); };
    const float A0 = s32(sv[0], qv[0]), A1 = s32(sv[1], qv[1]), A2 = s32(sv[2], qv[2]);
    float B = s16(A0, A1), C = s16(A2, A2);
    auto ror = [&](float v, auto n) { return v + asf(__builtin_amdgcn_update_dpp(0, asu(v), 0x120 + n.v, 0xf, 0xf, false)); };
    B = ror(B, IC<8>{}); C = ror(C, IC<8>{}); B = ror(B, IC<4>{}); C = ror(C, IC<4>{}); B = ror(B, IC<2>{}); C = ror(C, IC<2>{}); B = ror(B, IC<1>{}); C = ror(C, IC<1>{});
    const auto rb = __builtin_amdgcn_permlane32_swap(asu(B), asu(B), false, false), rc = __builtin_amdgcn_permlane32_swap(asu(C), asu(C), false, false);
    const float s = lane < 32 ? asf(rb[0]) : asf(rc[0]), ss = lane < 32 ? asf(rb[1]) : asf(rc[1]);
    const float mu = s * (1.0f / D), rs = __builtin_amdgcn_rsqf(fmaxf(__builtin_fmaf(-mu, mu, ss * (1.0f / D)), 0.f) + 1e-5f);
    const int j = q < 2 ? w + 4 * q : 8;
    if (tok == 0 && q < 3) stsc1(rsW, ST1_OFF + (u32)(g * 4096 + (9 * m + j) * 16), v4u{asu(mu), tag32, asu(rs), tag32});
  }

  const int l32 = lane & 31, hh = lane >> 5;
  const int qp = (lane & 15) >> 2, pp = lane & 3;
  {
    f32x16 aq = {}, ak = {}, av = {};
    int xa[4], wa1;
#pragma unroll
    for (int s4 = 0; s4 < 4; ++s4) xa[s4] = (16 + 32 * w + l32) * 128 + (((2 * s4 + hh) ^ ((l32 >> 1) & 7)) * 16);
    wa1 = P1_WOFF + (8 * hh + qp) * 192 + (q & 1) * 32 + pp * 8;
    constexpr float SC = 0.25f * 1.4426950408889634f, NEG = -3.0e38f;
    float bias0[4], bias1[4];
#pragma unroll
    for (int r = 0; r < 4; ++r) {
      const int d0 = tok + 16 - (4 * q + r), d1 = tok - (4 * q + r);
      bias0[r] = asf(opq(((d0 & 1) == 0 && d0 <= 14) ? 0 : (int)asu(NEG)));
      bias1[r] = asf(opq(((d1 & 1) == 0 && d1 >= 0 && d1 <= 14) ? 0 : (int)asu(NEG)));
    }
    struct P1F { bf16x8 xf; bf16x4 w[3][2]; };
    auto f_rd = [&](P1F& f, int u, int i) {
      const int sb = ((u >> 2) % 5) * P1_STAGE, s4 = u & 3;
      if (i == 0) f.xf = ldsb(xa[s4] + sb);
      else f.w[(i - 1) >> 1][(i - 1) & 1] = trr(wa1 + sb + s4 * 3072 + ((i - 1) >> 1) * 64 + ((i - 1) & 1) * 768);
    };
    P1F fa, fb;
    v4u st1;
    auto p1_step = [&](auto uc, P1F& f, P1F& n) {
      constexpr int u = uc.v;
      if constexpr ((u & 3) == 3 && u < 28) { WAITL0(); BAR(); }
      if constexpr (u == 24) ldsc1<0>(st1, rsW, ST1_OFF + (u32)(g * 4096 + (16 + 32 * w + l32) * 16), 0);
      sfor<0, 3>([&](auto ic) {
        constexpr int i = ic.v;
        const bf16x8 wf = cat(f.w[i][0], f.w[i][1]);
        if constexpr (i == 0) aq = mfma32(wf, f.xf, aq);
        if constexpr (i == 1) ak = mfma32(wf, f.xf, ak);
        if constexpr (i == 2) av = mfma32(f.xf, wf, av);
        if constexpr (u < 31) {
          if constexpr (i == 0) { f_rd(n, u + 1, 0); f_rd(n, u + 1, 1); f_rd(n, u + 1, 2); }
          if constexpr (i == 1) { f_rd(n, u + 1, 3); f_rd(n, u + 1, 4); }
          if constexpr (i == 2) { f_rd(n, u + 1, 5); f_rd(n, u + 1, 6); }
        }
        if constexpr (u == 29 && i == 2) {
          asm volatile("s_waitcnt vmcnt(0)" : "+v"(st1), "+v"(xres[0]), "+v"(xres[1]), "+v"(xres[2]), "+v"(xres[3])::"memory");
        }
        SCHED();
      });
    };
    BAR();
    sfor<0, 7>([&](auto i) { f_rd(fa, 0, i.v); });
    sfor<0, 15>([&](auto k) { p1_step(IC<2 * k.v>{}, fa, fb); p1_step(IC<2 * k.v + 1>{}, fb, fa); });
    for (u32 spins = 0; spins < (1u << 20); ++spins) {
      if (__all(st1.y == tag32 && st1.w == tag32)) break;
      __builtin_amdgcn_s_sleep(1);
      ldsc1<0>(st1, rsW, ST1_OFF + (u32)(g * 4096 + (16 + 32 * w + l32) * 16), 0);
      wait_for<0>(st1);
    }
    const float mean = asf(st1.x), rstd = asf(st1.z);
    LAS float* cs_ = lp<float>(M_CS);
    LAS float* mr = lp<float>(M_MR) + w * 64;
    if (lane < 32) { mr[lane] = mean; mr[32 + lane] = rstd; }
    p1_step(IC<30>{}, fa, fb);
    f32x4 mv[4], rv[4];
#pragma unroll
    for (int gg = 0; gg < 4; ++gg) { mv[gg] = *(LAS f32x4*)(mr + 8 * gg + 4 * hh); rv[gg] = *(LAS f32x4*)(mr + 32 + 8 * gg + 4 * hh); }
    p1_step(IC<31>{}, fb, fa);
    STAMP(1);
    BAR();
    f32x4 cq[4], ck[4];
#pragma unroll
    for (int gg = 0; gg < 4; ++gg) { cq[gg] = *(LAS f32x4*)(cs_ + 8 * gg + 4 * hh); ck[gg] = *(LAS f32x4*)(cs_ + 32 + 8 * gg + 4 * hh); }
    const float csv = cs_[64 + l32];
    SCHED();
    v2u qp_[2][2], kp[2][2], vp[2][2];
    {
      u32 pq[8], pk[8], pv[8];
#pragma unroll
      for (int gg = 0; gg < 4; ++gg) {
        float a[4], k4[4], v4[4];
#pragma unroll
        for (int r = 0; r < 4; ++r) {
          a[r] = __builtin_fmaf(-mean, cq[gg][r], aq[4 * gg + r]);
          k4[r] = rstd * __builtin_fmaf(-mean, ck[gg][r], ak[4 * gg + r]);
          v4[r] = rv[gg][r] * __builtin_fmaf(-mv[gg][r], csv, av[4 * gg + r]);
        }
        pq[2 * gg] = pack2(a[0], a[1]); pq[2 * gg + 1] = pack2(a[2], a[3]);
        pk[2 * gg] = pack2(k4[0], k4[1]); pk[2 * gg + 1] = pack2(k4[2], k4[3]);
        pv[2 * gg] = pack2(v4[0], v4[1]); pv[2 * gg + 1] = pack2(v4[2], v4[3]);
      }
#pragma unroll
      for (int x = 0; x < 2; ++x) {
#pragma unroll
        for (int d = 0; d < 2; ++d) {
          const auto sq_ = __builtin_amdgcn_permlane16_swap(pq[4 * x + d], pq[4 * x + 2 + d], false, false);
          qp_[0][x][d] = sq_[0]; qp_[1][x][d] = sq_[1];
          const auto sk_ = __builtin_amdgcn_permlane16_swap(pk[4 * x + d], pk[4 * x + 2 + d], false, false);
          kp[0][x][d] = sk_[0]; kp[1][x][d] = sk_[1];
          const auto s1 = __builtin_amdgcn_permlane32_swap(pv[4 * x + d], pv[4 * x + 2 + d], false, false);
          const auto s2 = __builtin_amdgcn_permlane16_swap(s1[0], s1[1], false, false);
          vp[x][0][d] = s2[0]; vp[x][1][d] = s2[1];
        }
      }
    }
    {
#pragma unroll
      for (int hd = 0; hd < 2; ++hd) {
        *lp<v2u>(KV_OFF + w * 2048 + hd * 512 + lane * 8) = kp[1][hd];
        *lp<v2u>(KV_OFF + w * 2048 + (2 + hd) * 512 + lane * 8) = vp[1][hd];
      }
    }
    WAITL0();
    BAR();
    v2u kprev[2], vprev[2];
    {
      const int pb = w == 0 ? KVH_OFF : KV_OFF + (w - 1) * 2048;
#pragma unroll
      for (int hd = 0; hd < 2; ++hd) {
        kprev[hd] = *lp<v2u>(pb + hd * 512 + lane * 8);
        vprev[hd] = *lp<v2u>(pb + (2 + hd) * 512 + lane * 8);
      }
    }
    const auto rq = __builtin_amdgcn_permlane16_swap(asu(rstd), asu(rstd), false, false);
    const v2u ones2 = {0x3F803F80u, 0x3F803F80u};
    auto attend = [&](int t, int hd, v2u kA, v2u vA, bool prev_ok) {
      const float sc = SC * asf(rq[t]);
      f32x4 s0 = mfma16k16(kA, qp_[t][hd], f32x4{});
      f32x4 s1 = mfma16k16(kp[t][hd], qp_[t][hd], f32x4{});
      float e[8];
#pragma unroll
      for (int r = 0; r < 4; ++r) {
        e[r] = __builtin_fmaf(s0[r], sc, prev_ok ? bias0[r] : NEG);
        e[4 + r] = __builtin_fmaf(s1[r], sc, bias1[r]);
      }
      float mx = fmaxf(fmaxf(fmaxf(e[0], e[1]), fmaxf(e[2], e[3])), fmaxf(fmaxf(e[4], e[5]), fmaxf(e[6], e[7])));
      mx = xmax32(xmax16(mx));
#pragma unroll
      for (int r = 0; r < 8; ++r) e[r] = __builtin_amdgcn_exp2f(e[r] - mx);
      const v2u p0 = {pack2(e[0], e[1]), pack2(e[2], e[3])}, p1 = {pack2(e[4], e[5]), pack2(e[6], e[7])};
      f32x4 sm = mfma16k16(ones2, p0, f32x4{});
      sm = mfma16k16(ones2, p1, sm);
      f32x4 o = mfma16k16(vA, p0, f32x4{});
      o = mfma16k16(vp[t][hd], p1, o);
      const float inv = __builtin_amdgcn_rcpf(sm[0]);
      return v2u{pack2(o[0] * inv, o[1] * inv), pack2(o[2] * inv, o[3] * inv)};
    };
    {
      const bool ok = T0 + 32 * w > 0;
      const v2u o10 = attend(1, 0, kp[0][0], vp[0][0], true), o11 = attend(1, 1, kp[0][1], vp[0][1], true);
      const v2u o00 = attend(0, 0, kprev[0], vprev[0], ok), o01 = attend(0, 1, kprev[1], vprev[1], ok);
      stsc1(rsW, O_OFF + (u32)(((g * 8 + 2 * w + 1) * 16 + m) * 1024 + lane * 16), v4u{o10.x, o10.y, o11.x, o11.y});
      stsc1(rsW, O_OFF + (u32)(((g * 8 + 2 * w) * 16 + m) * 1024 + lane * 16), v4u{o00.x, o00.y, o01.x, o01.y});
    }
  }
  STAMP(2);
  WAITVN(0);
  publish(fl, m, tag);
  STAMP(3);

  {
    bf16x8 w2[8][4];
    int a[4];
    {
      const int row = 4 * q + qp, f = ((row >> 1) & 3) << 1;
#pragma unroll
      for (int n = 0; n < 4; ++n) a[n] = opq(W2_OFF + row * 128 + (((2 * n + (pp >> 1)) ^ f) * 16) + (pp & 1) * 8);
#pragma unroll
      for (int s = 0; s < 8; ++s)
#pragma unroll
        for (int n = 0; n < 4; ++n) w2[s][n] = tr2(a[n] + s * 4096, 2048);
    }
    poll(fl, m, 16, tag);
    BAR();
    STAMP(4);
    v4u of[16];
    const u32 ob = O_OFF + (u32)((g * 8 + 4 * th + w) * 16) * 1024;
    sfor<0, 16>([&](auto i) { ldsc1<0>(of[i.v], rsW, lane * 16, ob + (u32)((i.v + 2 * ct) & 15) * 1024); });
    f32x4 acc[4] = {};
    sfor<0, 16>([&](auto sc) {
      constexpr int s = sc.v;
      wait_for<15 - s>(of[s]);
      const bf16x8 bfr = __builtin_bit_cast(bf16x8, of[s]);
#pragma unroll
      for (int n = 0; n < 4; ++n) {
        acc[n] = mfma16(w2[s & 7][n], bfr, acc[n]);
        if constexpr (s < 8) w2[s][n] = tr2(a[n] + (s + 8) * 4096, 2048);
      }
      SCHED();
    });
    STAMP(5);
    float s = 0.f, ss = 0.f;
    const int tl = 16 * w + tok;
    const int t32 = g * 4 + 2 * th + (w >> 1);
    const u32 hoff = H_OFF + (u32)((t32 * 32 + 4 * ct) * 1024 + (16 * (w & 1) + tok + 32 * (q >> 1)) * 16 + (q & 1) * 8);
#pragma unroll
    for (int n = 0; n < 4; ++n) {
      const u32 h01 = pack2(bflo(xres[n].x) + acc[n][0], bfhi(xres[n].x) + acc[n][1]);
      const u32 h23 = pack2(bflo(xres[n].y) + acc[n][2], bfhi(xres[n].y) + acc[n][3]);
      const v2u hp = {h01, h23};
      stsc1_8(rsW, hoff + n * 1024, hp);
      *lp<v2u>(M_HK + tl * HK_PITCH + (16 * n + 4 * q) * 2) = hp;
      const float a0 = bflo(h01), a1 = bfhi(h01), a2 = bflo(h23), a3 = bfhi(h23);
      s += (a0 + a1) + (a2 + a3);
      ss = __builtin_fmaf(a0, a0, ss); ss = __builtin_fmaf(a1, a1, ss); ss = __builtin_fmaf(a2, a2, ss); ss = __builtin_fmaf(a3, a3, ss);
    }
    s = xadd32(xadd16(s)); ss = xadd32(xadd16(ss));
    if (q == 0) stsc1_8(rsW, ST_OFF + (u32)((ct * S + T0 + 64 * th + tl) * 8), v2u{asu(s), asu(ss)});
  }
  WAITVN(0);
  WAITL0();
  publish(fl + 256, m, tag);
  STAMP(6);

  {
    const int wa = (8 * hh + qp) * 192 + (q & 1) * 32 + pp * 8;
    bf16x8 wA[6], wB[6];
    auto w3_read1 = [&](bf16x8& dst, int base, int i) { const int o = base + (i & 1) * 12288 + (i >> 1) * 64; dst = tr2(o, 768); };
    poll(fl + 256, m, 16, tag);
    BAR();
    STAMP(7);
    const int t32 = g * 4 + w;
    v2u st[4];
#pragma unroll
    for (int i = 0; i < 4; ++i) ldsc1_8(st[i], rsW, (u32)(((hh * 4 + i) * S + l32) * 8), ST_OFF + (u32)(t32 * 32) * 8);
    v4u hf[16];
    const u32 hb = H_OFF + (u32)(t32 * 32) * 1024;
    auto ldh = [&](auto i) { ldsc1<(i.v & 3) * 1024>(hf[i.v & 15], rsW, lane * 16, hb + (u32)(((i.v >> 2) + m) & 7) * 4096); };
    sfor<0, 16>(ldh);
#pragma unroll
    for (int i = 0; i < 6; ++i) w3_read1(wA[i], wa, i);
    asm volatile("s_waitcnt vmcnt(16)" : "+v"(st[0]), "+v"(st[1]), "+v"(st[2]), "+v"(st[3])::"memory");
    float rs, nb;
    {
      float s = (asf(st[0].x) + asf(st[1].x)) + (asf(st[2].x) + asf(st[3].x)), ss = (asf(st[0].y) + asf(st[1].y)) + (asf(st[2].y) + asf(st[3].y));
      s = xadd32(s); ss = xadd32(ss);
      const float mu = s * (1.0f / D);
      rs = __builtin_amdgcn_rsqf(fmaxf(__builtin_fmaf(-mu, mu, ss * (1.0f / D)), 0.f) + 1e-5f);
      nb = -mu * rs;
    }
    auto ln = [&](v4u h, int d) {
      const f32x2 v = {bflo(h[d]), bfhi(h[d])};
      const f32x2 r = __builtin_elementwise_fma(v, f32x2{rs, rs}, f32x2{nb, nb});
      return pack2(r[0], r[1]);
    };
    v4u yA, yB;
    wait_for<15>(hf[0]);
#pragma unroll
    for (int d = 0; d < 4; ++d) yA[d] = ln(hf[0], d);
    ldh(IC<16>{});
    f32x16 acc[6] = {};
    auto step = [&](auto sc, bf16x8(&wc)[6], bf16x8(&wn)[6], v4u& yc, v4u& yn) {
      constexpr int s = sc.v, c1 = (s + 1) >> 2;
      if constexpr (s + 1 < 32) wait_for<(16 + s < 31 ? 16 + s : 31) - (s + 1)>(hf[(s + 1) & 15]);
      if constexpr (s >= 4 && (s & 3) == 0) BAR();
      const int base = opq(wa + (c1 % 6) * SLOT + (16 * ((s + 1) & 3)) * 192);
      const bf16x8 yb = __builtin_bit_cast(bf16x8, yc);
      __builtin_amdgcn_s_waitcnt(0xC07F);
      sfor<0, 6>([&](auto ic) {
        constexpr int i = ic.v;
        acc[i] = mfma32(wc[i], yb, acc[i]);
        if constexpr (s + 1 < 32) {
          if constexpr (i < 3) { w3_read1(wn[2 * i], base, 2 * i); w3_read1(wn[2 * i + 1], base, 2 * i + 1); }
          if constexpr (i >= 2) yn[i - 2] = ln(hf[(s + 1) & 15], i - 2);
        }
        if constexpr (i == 5 && 17 + s < 32) ldh(IC<(17 + s) & 31>{});
        SCHED();
      });
    };
    sfor<0, 16>([&](auto k) { step(IC<2 * k.v>{}, wA, wB, yA, yB); step(IC<2 * k.v + 1>{}, wB, wA, yB, yA); });
    STAMP(8);
    BAR();
#pragma unroll
    for (int i = 0; i < 3; ++i) {
      const int f0 = 96 * m + 32 * i;
      if (f0 < F) {
#pragma unroll
        for (int s = 0; s < 2; ++s) {
          v4u o;
#pragma unroll
          for (int d = 0; d < 4; ++d) {
            const int r = 8 * s + 2 * d;
            o[d] = pack2(fmaxf(acc[2 * i][r], 0.f) * acc[2 * i + 1][r], fmaxf(acc[2 * i][r + 1], 0.f) * acc[2 * i + 1][r + 1]);
          }
          stsc1(rsW, A_OFF + (u32)((t32 * 86 + f0 / 16 + s) * 1024 + lane * 16), o);
        }
      }
    }
    WAITVN(0);
  }
  publish(fl + 512, m, tag);
  STAMP(9);

  {
    poll(fl + 512, m, 15, tag);
    BAR();
    STAMP(10);
    const int mh = w & 1, kh = w >> 1;
    const int t32 = g * 4 + 2 * th + mh;
    v4u af[36];
    const u32 ab = A_OFF + (u32)(t32 * 86 + kh * 43) * 1024;
    auto lda = [&](auto i) { const int ps = i.v + 5 * ct; ldsc1<0>(af[i.v % 36], rsW, lane * 16, ab + (u32)(ps >= 43 ? ps - 43 : ps) * 1024); };
    sfor<0, 14>(lda);
    f32x16 acc[2] = {};
    int wa[2];
#pragma unroll
    for (int n = 0; n < 2; ++n) wa[n] = (4 * hh + qp) * 128 + ((n ^ ((qp >> 1) & 1)) * 64) + (q & 1) * 32 + pp * 8;
    bf16x8 wA[2], wB[2];
    auto w4_read = [&](bf16x8(&dst)[2], auto sc) {
      constexpr int s = sc.v;
      const int off = s < 42 ? ((s / 6 + 2) % 6) * SLOT + kh * 12288 + (s % 6) * 2048 : M_W4T + kh * 2048;
#pragma unroll
      for (int n = 0; n < 2; ++n) dst[n] = tr2(opq(wa[n] + off), 1024);
    };
    w4_read(wA, IC<0>{});
    auto step = [&](auto sc, bf16x8(&wc)[2], bf16x8(&wn)[2]) {
      constexpr int s = sc.v;
      constexpr int issued = (14 + 2 * (s < 8 ? s : 8) + (s > 8 ? s - 8 : 0)) < 43 ? (14 + 2 * (s < 8 ? s : 8) + (s > 8 ? s - 8 : 0)) : 43;
      wait_for<issued - 1 - s>(af[s % 36]);
      if constexpr (s == 6 || s == 30) BAR();
      if constexpr (s + 1 < 43) w4_read(wn, IC<s + 1>{});
      const bf16x8 bfr = __builtin_bit_cast(bf16x8, af[s % 36]);
      acc[0] = mfma32(wc[0], bfr, acc[0]);
      if constexpr (issued < 43) lda(IC<(issued < 43 ? issued : 0)>{});
      SCHED();
      acc[1] = mfma32(wc[1], bfr, acc[1]);
      if constexpr (s < 8 && issued + 1 < 43) lda(IC<(issued + 1 < 43 ? issued + 1 : 0)>{});
      SCHED();
    };
    sfor<0, 21>([&](auto k) { step(IC<2 * k.v>{}, wA, wB); step(IC<2 * k.v + 1>{}, wB, wA); });
    step(IC<42>{}, wA, wB);
    STAMP(11);
    LAS f32x4* red = lp<f32x4>(3 * SLOT);
    {
      const f32x16 snd = kh ? acc[0] : acc[1];
#pragma unroll
      for (int gg = 0; gg < 4; ++gg) red[(w * 4 + gg) * 64 + lane] = f32x4{snd[4 * gg], snd[4 * gg + 1], snd[4 * gg + 2], snd[4 * gg + 3]};
    }
    if (m == 0 && tid == 0) __hip_atomic_store(ep, tag, RLX_AGENT);
    WAITL0(); BAR();
    {
      const f32x16 keep = kh ? acc[1] : acc[0];
      const int pw = w ^ 2;
      GAS u16* orow = (GAS u16*)p.out + (size_t)(T0 + 64 * th + 32 * mh + l32) * D + 64 * ct + 32 * kh + 4 * hh;
#pragma unroll
      for (int gg = 0; gg < 4; ++gg) {
        const f32x4 o = red[(pw * 4 + gg) * 64 + lane];
        const v2u hk = *lp<v2u>(M_HK + (32 * mh + l32) * HK_PITCH + (32 * kh + 8 * gg + 4 * hh) * 2);
        v2u r;
        r.x = pack2(bflo(hk.x) + (keep[4 * gg] + o[0]), bfhi(hk.x) + (keep[4 * gg + 1] + o[1]));
        r.y = pack2(bflo(hk.y) + (keep[4 * gg + 2] + o[2]), bfhi(hk.y) + (keep[4 * gg + 3] + o[3]));
        *(GAS v2u*)(orow + 8 * gg) = r;
      }
    }
  }
  STAMP(12);
}

extern "C" void kernel_launch(void* const* d_in, const int* in_sizes, int n_in, void* d_out, int out_size,
                              void* d_ws, size_t ws_size, hipStream_t stream) {
  Params p;
  p.x = (const u16*)d_in[0]; p.wqkv = (const u16*)d_in[1]; p.wo = (const u16*)d_in[2];
  p.wg = (const u16*)d_in[4]; p.wu = (const u16*)d_in[5]; p.wd = (const u16*)d_in[6];
  p.out = (u16*)d_out; p.ws = (unsigned char*)d_ws;
  (void)hipFuncSetAttribute((const void*)fused_block, hipFuncAttributeMaxDynamicSharedMemorySize, LDS_BYTES);
  fused_block<<<256, 512, LDS_BYTES, stream>>>(p);
}
```

```cpp
#include <hip/hip_runtime.h>
#include <stdint.h>

typedef unsigned short u16;
typedef unsigned u32;
typedef unsigned long long u64;
typedef short bf16x8 __attribute__((ext_vector_type(8)));
typedef short bf16x4 __attribute__((ext_vector_type(4)));
typedef float f32x2 __attribute__((ext_vector_type(2)));
typedef float f32x4 __attribute__((ext_vector_type(4)));
typedef float f32x16 __attribute__((ext_vector_type(16)));
typedef unsigned v4u __attribute__((ext_vector_type(4)));
typedef unsigned v2u __attribute__((ext_vector_type(2)));
typedef __bf16 bf2_t __attribute__((ext_vector_type(2)));
#define GAS __attribute__((address_space(1)))
#define LAS __attribute__((address_space(3)))
#define DI __device__ __forceinline__
#define RLX_AGENT __ATOMIC_RELAXED, __HIP_MEMORY_SCOPE_AGENT
typedef GAS u64 gu64;
typedef __amdgpu_buffer_rsrc_t rsrc_t;

constexpr int S = 2048, D = 512, F = 1376;
constexpr u32 O_OFF = 0;
constexpr u32 H_OFF = 2u << 20;
constexpr u32 A_OFF = 4u << 20;
constexpr u32 ST_OFF = 10u << 20;
constexpr u32 ST1_OFF = (10u << 20) + (512u << 10);
constexpr u32 FL_OFF = 11u << 20;
constexpr u32 EP_OFF = 12u << 20;
constexpr u32 WS_BYTES = 13u << 20;

constexpr int SLOT = 24576;
constexpr int MISC = 6 * SLOT;
constexpr int LDS_BYTES = 163840;
constexpr int P1_STAGE = 30720;
constexpr int P1_WOFF = 18432;
constexpr int W2_OFF = 3 * P1_STAGE;
constexpr int M_CS = 157696;
constexpr int M_MR = M_CS + 1024;
constexpr int KVH_OFF = M_CS + 2048;
constexpr int KV_OFF = 3 * SLOT;
constexpr int M_HK = MISC;
constexpr int M_W4T = MISC + 8704;
constexpr int HK_PITCH = 136;

struct Params {
  const u16* x; const u16* wqkv; const u16* wo; const u16* wg; const u16* wu; const u16* wd;
  u16* out; unsigned char* ws;
};

template <int I> struct IC { static constexpr int v = I; };
template <int I, int N, class Fn> DI void sfor(Fn&& f) { if constexpr (I < N) { f(IC<I>{}); sfor<I + 1, N>(f); } }

DI u32 pack2(float a, float b) { f32x2 v = {a, b}; bf2_t r = __builtin_convertvector(v, bf2_t); return __builtin_bit_cast(u32, r); }
DI float bflo(u32 u) { return __builtin_bit_cast(float, u << 16); }
DI float bfhi(u32 u) { return __builtin_bit_cast(float, u & 0xffff0000u); }
DI float asf(u32 u) { return __builtin_bit_cast(float, u); }
DI u32 asu(float f) { return __builtin_bit_cast(u32, f); }
DI f32x4 mfma16(bf16x8 a, bf16x8 b, f32x4 c) { return __builtin_amdgcn_mfma_f32_16x16x32_bf16(a, b, c, 0, 0, 0); }
DI f32x4 mfma16k16(v2u a, v2u b, f32x4 c) { return __builtin_amdgcn_mfma_f32_16x16x16bf16_1k(__builtin_bit_cast(bf16x4, a), __builtin_bit_cast(bf16x4, b), c, 0, 0, 0); }
DI f32x16 mfma32(bf16x8 a, bf16x8 b, f32x16 c) { return __builtin_amdgcn_mfma_f32_32x32x16_bf16(a, b, c, 0, 0, 0); }
DI bf16x8 cat(bf16x4 a, bf16x4 b) { return __builtin_shufflevector(a, b, 0, 1, 2, 3, 4, 5, 6, 7); }
#pragma clang diagnostic ignored "-Wint-to-pointer-cast"
DI bf16x4 trr(int off) { return __builtin_amdgcn_ds_read_tr16_b64_v4i16((LAS bf16x4*)(u32)off); }
DI bf16x8 tr2(int off, int d) { return cat(trr(off), trr(off + d)); }
DI bf16x8 ldsb(int off) { return *(LAS bf16x8*)(u32)off; }
template <class T> DI LAS T* lp(int off) { return (LAS T*)(u32)off; }
DI rsrc_t mkrs(const void* p, u32 bytes) { return __builtin_amdgcn_make_buffer_rsrc((void*)p, 0, bytes, 0x00020000); }
DI u32 rfl(u32 v) { return __builtin_amdgcn_readfirstlane(v); }
DI int opq(int v) { asm volatile("" : "+v"(v)); return v; }
DI float xmax16(float v) { auto r = __builtin_amdgcn_permlane16_swap(asu(v), asu(v), false, false); return fmaxf(asf(r[0]), asf(r[1])); }
DI float xmax32(float v) { auto r = __builtin_amdgcn_permlane32_swap(asu(v), asu(v), false, false); return fmaxf(asf(r[0]), asf(r[1])); }
DI float xadd16(float v) { auto r = __builtin_amdgcn_permlane16_swap(asu(v), asu(v), false, false); return asf(r[0]) + asf(r[1]); }
DI float xadd32(float v) { auto r = __builtin_amdgcn_permlane32_swap(asu(v), asu(v), false, false); return asf(r[0]) + asf(r[1]); }
#define BAR() __builtin_amdgcn_s_barrier()
#define SCHED() __builtin_amdgcn_sched_barrier(0)
#define WAITVN(n) asm volatile("s_waitcnt vmcnt(%0)" ::"i"(n) : "memory")
#define WAITL0() asm volatile("s_waitcnt lgkmcnt(0)" ::: "memory")

DI void dma(rsrc_t rs, u32 voff, u32 soff, u32 ldsaddr) {
  asm volatile("s_mov_b32 m0, %0\n\ts_nop 0\n\tbuffer_load_dwordx4 %1, %2, %3 offen lds" ::"s"(ldsaddr), "v"(voff), "s"(rs), "s"(soff) : "memory");
}
template <int IMM> DI void ldsc1(v4u& dst, rsrc_t rs, u32 voff, u32 soff) {
  asm volatile("buffer_load_dwordx4 %0, %1, %2, %3 offen offset:%4 sc1" : "=v"(dst) : "v"(voff), "s"(rs), "s"(soff), "i"(IMM) : "memory");
}
DI void ldsc1_8(v2u& dst, rsrc_t rs, u32 voff, u32 soff) {
  asm volatile("buffer_load_dwordx2 %0, %1, %2, %3 offen sc1" : "=v"(dst) : "v"(voff), "s"(rs), "s"(soff) : "memory");
}
DI void ld16(v4u& dst, rsrc_t rs, u32 voff, u32 soff) {
  asm volatile("buffer_load_dwordx4 %0, %1, %2, %3 offen" : "=v"(dst) : "v"(voff), "s"(rs), "s"(soff) : "memory");
}
DI void ld4_keep(u32& dst, rsrc_t rs, u32 voff) { asm volatile("buffer_load_dword %0, %1, %2, 0 offen" : "+v"(dst) : "v"(voff), "s"(rs) : "memory"); }
template <int IMM> DI void ld8(v2u& dst, rsrc_t rs, u32 voff, u32 soff) {
  asm volatile("buffer_load_dwordx2 %0, %1, %2, %3 offen offset:%4" : "=v"(dst) : "v"(voff), "s"(rs), "s"(soff), "i"(IMM) : "memory");
}
template <int N> DI void wait_for(v4u& a) { asm volatile("s_waitcnt vmcnt(%1)" : "+v"(a) : "i"(N > 63 ? 63 : N) : "memory"); }
DI void stsc1(rsrc_t rs, u32 off, v4u v) { __builtin_amdgcn_raw_buffer_store_b128(v, rs, off, 0, 16); }
DI void stsc1_8(rsrc_t rs, u32 off, v2u v) { __builtin_amdgcn_raw_buffer_store_b64(v, rs, off, 0, 16); }

#define STAMP(i) do {} while (0)

DI void publish(gu64* fl, int m, u64 tag) {
  BAR();
  if (threadIdx.x < 16) __hip_atomic_store(fl + threadIdx.x * 16 + m, tag, RLX_AGENT);
}
DI void poll(gu64* fl, int m, int nprod, u64 tag) {
  if (threadIdx.x < 64) {
    const int lane = threadIdx.x;
    for (u32 spins = 0; spins < (1u << 20); ++spins) {
      u64 v = tag;
      if (lane < nprod) v = __hip_atomic_load(fl + m * 16 + lane, RLX_AGENT);
      if (__all(v == tag)) break;
    }
    __builtin_amdgcn_fence(__ATOMIC_ACQUIRE, "wavefront");
  }
}

__global__ __launch_bounds__(512, 2) void fused_block(Params p) {
  extern __shared__ __attribute__((aligned(16))) unsigned char smem[];
  const int tid = threadIdx.x, lane = tid & 63, w8 = (int)rfl(tid >> 6), w = w8 & 3;
  const int b = blockIdx.x, g = (b & 7) * 2 + ((b >> 3) >> 4), m = (b >> 3) & 15;
  const int T0 = g * 128;
  const int tok = lane & 15, q = lane >> 4;
  const int th = m >> 3, ct = m & 7;
  const rsrc_t rsW = mkrs(p.ws, WS_BYTES);
  const rsrc_t rsX = mkrs(p.x, S * D * 2);
  const u32 halo0 = (u32)(T0 >= 16 ? T0 - 16 : 0);
  asm volatile("" ::"s"((u32)(size_t)smem));

  if (w8 >= 4) {
    const rsrc_t rsW1 = mkrs(p.wqkv, D * 3 * D * 2);
    v2u epv;
    ldsc1_8(epv, rsW, 0, EP_OFF + g * 128);
    u32 vx0, vx1, vxh, vw0, vw1, vw2;
    {
      const int r8 = lane >> 3, sl = lane & 7;
      vx0 = r8 * 1024 + ((sl ^ ((lane >> 4) & 7)) * 16);
      vx1 = r8 * 1024 + ((sl ^ ((4 + (lane >> 4)) & 7)) * 16);
      vxh = (w & 1) ? vx1 : vx0;
      auto pat = [&](int j) { const int e = j * 64 + lane, r = e / 12, pc = e % 12; return (u32)(r * 3072 + (pc >> 2) * 1024 + (pc & 3) * 16); };
      vw0 = pat(0); vw1 = pat(1); vw2 = pat(2);
    }
    const u32 sxh = (halo0 + 8 * (w & 1)) * 1024, sxm = (u32)(T0 + 32 * w) * 1024, sw1 = (u32)(16 * w) * 3072 + m * 64;
    const int rot = (g + m) & 7;
    auto p1_piece = [&](int cc, int j) {
      const u32 lb = (cc % 5) * P1_STAGE;
      const u32 c = (u32)((cc + rot) & 7);
      if (j == 0) dma(rsX, vxh, sxh + c * 128, lb + (w & 1) * 1024);
      if (j == 1) dma(rsW1, vw0, sw1 + c * (64 * 3072), lb + P1_WOFF + (3 * w) * 1024);
      if (j == 2) dma(rsX, vx0, sxm + c * 128, lb + (2 + 4 * w) * 1024);
      if (j == 3) dma(rsW1, vw1, sw1 + c * (64 * 3072), lb + P1_WOFF + (3 * w + 1) * 1024);
      if (j == 4) dma(rsX, vx1, sxm + c * 128 + 8192, lb + (3 + 4 * w) * 1024);
      if (j == 5) dma(rsW1, vw2, sw1 + c * (64 * 3072), lb + P1_WOFF + (3 * w + 2) * 1024);
      if (j == 6) dma(rsX, vx0, sxm + c * 128 + 16384, lb + (4 + 4 * w) * 1024);
      if (j == 7) dma(rsX, vx1, sxm + c * 128 + 24576, lb + (5 + 4 * w) * 1024);
    };
    const rsrc_t rsW2 = mkrs(p.wo, D * D * 2), rsG = mkrs(p.wg, D * F * 2), rsU = mkrs(p.wu, D * F * 2), rsD = mkrs(p.wd, F * D * 2);
    const u32 v2 = (lane >> 3) * 1024 + (((lane & 7) ^ (((lane >> 4) & 3) << 1)) * 16);
    auto w2_piece = [&](int i) {
      const int ii = 16 * w + i, ls = ii >> 2;
      dma(rsW2, v2, (u32)(32 * ((ls + 2 * ct) & 15) + 8 * (ii & 3)) * 1024 + ct * 128, W2_OFF + ii * 1024);
    };
    const int qp = (lane & 15) >> 2, pp = lane & 3;
    f32x4 ahA = {}, ahB = {};
    const int xha0 = tok * 128 + ((q ^ (tok >> 1)) * 16), xha1 = tok * 128 + (((4 + q) ^ (tok >> 1)) * 16);
    const int wha = P1_WOFF + (8 * q + qp) * 192 + (1 + (w >> 1)) * 64 + (w & 1) * 32 + pp * 8;
    const int whb = P1_WOFF + (8 * q + qp) * 192 + (w < 2 ? 0 : 128) + (w & 1) * 32 + pp * 8;
    auto halo_with = [&](int hc, auto&& issue) {
      const int sb = (hc % 5) * P1_STAGE;
      v4u xh[2]; bf16x8 fa[2], fb[2];
#pragma unroll
      for (int s2 = 0; s2 < 2; ++s2) {
        xh[s2] = __builtin_bit_cast(v4u, ldsb((s2 ? xha1 : xha0) + sb));
        fa[s2] = tr2(wha + sb + s2 * 6144, 768); fb[s2] = tr2(whb + sb + s2 * 6144, 768);
      }
      SCHED();
      issue(0); issue(1); issue(2); issue(3);
      SCHED();
#pragma unroll
      for (int s2 = 0; s2 < 2; ++s2) {
#pragma unroll
        for (int d = 0; d < 4; ++d) xh[s2][d] = tok == 0 ? 0x3F803F80u : xh[s2][d];
        const bf16x8 xhb = __builtin_bit_cast(bf16x8, xh[s2]);
        ahA = mfma16(fa[s2], xhb, ahA);
        ahB = mfma16(fb[s2], xhb, ahB);
      }
      SCHED();
      issue(4); issue(5); issue(6); issue(7);
    };
    sfor<0, 24>([&](auto i) { p1_piece(i.v >> 3, i.v & 7); });
    WAITVN(16); BAR();
    halo_with(0, [&](int j) { p1_piece(3, j); });
    WAITVN(16); BAR();
    halo_with(1, [&](int j) { p1_piece(4, j); });
    WAITVN(16); BAR();
    halo_with(2, [&](int j) { p1_piece(5, j); });
    WAITVN(16); BAR();
    halo_with(3, [&](int j) { p1_piece(6, j); });
    WAITVN(16); BAR();
    halo_with(4, [&](int j) { p1_piece(7, j); });
    u32 touch = 0;
    {
      const u32 wg_ = (u32)(b * 4 + w);
      const u32 o4 = (u32)lane < 4u ? (wg_ * 4u + lane) * 128u : 0xFFFFFF00u, o11 = (u32)lane < 11u ? (wg_ * 11u + lane) * 128u : 0xFFFFFF00u;
      ld4_keep(touch, rsW2, o4); ld4_keep(touch, rsG, o11); ld4_keep(touch, rsU, o11); ld4_keep(touch, rsD, o11);
    }
    v4u sth;
    ldsc1<0>(sth, rsW, ST1_OFF + (u32)(g * 4096 + tok * 16), 0);
    WAITVN(21); BAR();
    halo_with(5, [&](int) {});
    WAITVN(13); BAR();
    halo_with(6, [&](int) {});
    WAITVN(5); BAR();
    halo_with(7, [&](int j) { w2_piece(j); });
    asm volatile("s_waitcnt vmcnt(8)" : "+v"(sth), "+v"(touch), "+v"(epv)::"memory");
    const u64 tag = (((u64)rfl(epv.y) << 32) | rfl(epv.x)) + 1;
    const u32 tag32 = (u32)tag ^ (u32)(tag >> 32) ^ 0x9E3779B9u;
    for (u32 spins = 0; spins < (1u << 20); ++spins) {
      if (__all(sth.y == tag32 && sth.w == tag32)) break;
      __builtin_amdgcn_s_sleep(1);
      ldsc1<0>(sth, rsW, ST1_OFF + (u32)(g * 4096 + tok * 16), 0);
      wait_for<0>(sth);
    }
    {
      LAS float* cs_ = lp<float>(M_CS);
      if (tok == 0) {
        *(LAS f32x4*)(cs_ + 32 + 16 * w + 4 * q) = ahA;
        if (w < 2) *(LAS f32x4*)(cs_ + 16 * w + 4 * q) = ahB;
      }
      const float hmu = asf(sth.x), hrs = asf(sth.z);
      float hv[4];
#pragma unroll
      for (int r = 0; r < 4; ++r) {
        const float c = asf(__builtin_amdgcn_update_dpp(0, asu(ahA[r]), 0x150, 0xf, 0xf, false));
        hv[r] = hrs * __builtin_fmaf(-hmu, c, ahA[r]);
      }
      const u32 o0 = pack2(hv[0], hv[1]), o1 = pack2(hv[2], hv[3]);
      if (w < 2) *lp<v2u>(KVH_OFF + w * 512 + (tok + 16 * (((q & 1) << 1) | (q >> 1))) * 8) = v2u{o0, o1};
      else {
        LAS u16* vv = lp<u16>(KVH_OFF + w * 512) + (4 * q + 16 * (tok >> 2)) * 4 + (tok & 3);
        vv[0] = (u16)o0; vv[4] = (u16)(o0 >> 16); vv[8] = (u16)o1; vv[12] = (u16)(o1 >> 16);
      }
    }
    WAITL0();
    const u32 v30 = (lane / 12) * (F * 2) + (lane % 12) * 16, v31 = ((64 + lane) / 12) * (F * 2) + ((64 + lane) % 12) * 16,
              v32 = ((128 + lane) / 12) * (F * 2) + ((128 + lane) % 12) * 16;
    const u32 s3 = (u32)(16 * w) * (F * 2) + m * 192;
    auto w3_chunk = [&](int c) {
      const u32 so = s3 + (u32)(64 * ((c + m) & 7)) * (F * 2), lb = (c % 6) * SLOT + 3 * w * 1024;
      dma(rsG, v30, so, lb); dma(rsG, v31, so, lb + 1024); dma(rsG, v32, so, lb + 2048);
      dma(rsU, v30, so, lb + 12288); dma(rsU, v31, so, lb + 12288 + 1024); dma(rsU, v32, so, lb + 12288 + 2048);
    };
    const u32 v4s = (lane >> 3) * 1024 + (((lane & 7) ^ (((lane >> 4) & 1) << 2)) * 16);
    auto w4_row = [&](int ls) { const int ps = ls + 5 * ct; return (u32)((ps >= 43 ? ps - 43 : ps) * 16); };
    auto w4_part = [&](int part) {
      const u32 lb = ((part + 2) % 6) * SLOT + 3 * w * 1024;
#pragma unroll
      for (int j = 0; j < 3; ++j) {
        const int jj = 3 * w + j;
        const u32 so = (w4_row(6 * part + (jj >> 1)) + (jj & 1) * 8) * 1024 + ct * 128;
        dma(rsD, v4s, so, lb + j * 1024); dma(rsD, v4s, so + 688 * 1024, lb + 12288 + j * 1024);
      }
    };
    BAR();
    sfor<8, 16>([&](auto i) { w2_piece(i.v); });
    BAR();
    w3_chunk(0); w3_chunk(1);
    WAITVN(12);
    BAR();
    w3_chunk(2);
    BAR();
    BAR();
    w3_chunk(3);
    WAITVN(6); BAR();
    w3_chunk(4); w3_chunk(5);
    BAR(); w3_chunk(6);
    WAITVN(18); BAR(); w3_chunk(7);
    WAITVN(18); BAR(); w4_part(0);
    WAITVN(18); BAR(); w4_part(1);
    WAITVN(18); BAR(); w4_part(2);
    WAITVN(18); BAR(); w4_part(3);
    BAR(); w4_part(4);
    BAR();
    w4_part(5);
    dma(rsD, v4s, ((u32)(w >> 1) * 688 + w4_row(42) + (w & 1) * 8) * 1024 + ct * 128, M_W4T + w * 1024);
    BAR();
    WAITVN(7); BAR();
    WAITVN(0); BAR(); w4_part(6);
    WAITVN(0); BAR();
    BAR();
    return;
  }

  __builtin_amdgcn_s_setprio(2);
  STAMP(0);
  gu64* ep = (gu64*)(p.ws + EP_OFF) + g * 16;
  gu64* fl = (gu64*)(p.ws + FL_OFF) + g * 768;
  v2u epv, xres[4];
  v4u srow[3];
  ldsc1_8(epv, rsW, 0, EP_OFF + g * 128);
  sfor<0, 3>([&](auto jc) {
    const int j = jc.v < 2 ? w + 4 * jc.v : 8, R = 9 * m + j;
    const u32 token = R < 16 ? halo0 + R : (u32)(T0 + R - 16);
    ld16(srow[jc.v], rsX, lane * 16, token * 1024);
  });
  {
    const u32 vo = (u32)tok * 1024 + q * 8, so = (u32)(T0 + 64 * th + 16 * w) * 1024 + ct * 128;
    ld8<0>(xres[0], rsX, vo, so); ld8<32>(xres[1], rsX, vo, so); ld8<64>(xres[2], rsX, vo, so); ld8<96>(xres[3], rsX, vo, so);
  }
  asm volatile("s_waitcnt vmcnt(4)" : "+v"(epv), "+v"(srow[0]), "+v"(srow[1]), "+v"(srow[2])::"memory");
  const u64 tag = (((u64)rfl(epv.y) << 32) | rfl(epv.x)) + 1;
  const u32 tag32 = (u32)tag ^ (u32)(tag >> 32) ^ 0x9E3779B9u;
    {
    float sv[3], qv[3];
#pragma unroll
    for (int j = 0; j < 3; ++j) {
      float s = 0.f, ss = 0.f;
#pragma unroll
      for (int d = 0; d < 4; ++d) {
        const float a = bflo(srow[j][d]), c = bfhi(srow[j][d]);
        s += a + c; ss = __builtin_fmaf(a, a, ss); ss = __builtin_fmaf(c, c, ss);
      }
      sv[j] = s; qv[j] = ss;
    }
    auto s32 = [&](float a, float c) { auto r = __builtin_amdgcn_permlane32_swap(asu(a), asu(c), false, false); return asf(r[0]) + asf(r[1]); };
    auto s16 = [&](float a, float c) { auto r = __builtin_amdgcn_permlane16_swap(asu(a), asu(c), false, false); return asf(r[0]) + asf(r[1]); };
    const float A0 = s32(sv[0], qv[0]), A1 = s32(sv[1], qv[1]), A2 = s32(sv[2], qv[2]);
    float B = s16(A0, A1), C = s16(A2, A2);
    auto ror = [&](float v, auto n) { return v + asf(__builtin_amdgcn_update_dpp(0, asu(v), 0x120 + n.v, 0xf, 0xf, false)); };
    B = ror(B, IC<8>{}); C = ror(C, IC<8>{}); B = ror(B, IC<4>{}); C = ror(C, IC<4>{}); B = ror(B, IC<2>{}); C = ror(C, IC<2>{}); B = ror(B, IC<1>{}); C = ror(C, IC<1>{});
    const auto rb = __builtin_amdgcn_permlane32_swap(asu(B), asu(B), false, false), rc = __builtin_amdgcn_permlane32_swap(asu(C), asu(C), false, false);
    const float s = lane < 32 ? asf(rb[0]) : asf(rc[0]), ss = lane < 32 ? asf(rb[1]) : asf(rc[1]);
    const float mu = s * (1.0f / D), rs = __builtin_amdgcn_rsqf(fmaxf(__builtin_fmaf(-mu, mu, ss * (1.0f / D)), 0.f) + 1e-5f);
    const int j = q < 2 ? w + 4 * q : 8;
    if (tok == 0 && q < 3) stsc1(rsW, ST1_OFF + (u32)(g * 4096 + (9 * m + j) * 16), v4u{asu(mu), tag32, asu(rs), tag32});
  }

  const int l32 = lane & 31, hh = lane >> 5;
  const int qp = (lane & 15) >> 2, pp = lane & 3;
  {
    f32x16 aq = {}, ak = {}, av = {};
    int xa[4], wa1;
#pragma unroll
    for (int s4 = 0; s4 < 4; ++s4) xa[s4] = (16 + 32 * w + l32) * 128 + (((2 * s4 + hh) ^ ((l32 >> 1) & 7)) * 16);
    wa1 = P1_WOFF + (8 * hh + qp) * 192 + (q & 1) * 32 + pp * 8;
    constexpr float SC = 0.25f * 1.4426950408889634f, NEG = -3.0e38f;
    float bias0[4], bias1[4];
#pragma unroll
    for (int r = 0; r < 4; ++r) {
      const int d0 = tok + 16 - (4 * q + r), d1 = tok - (4 * q + r);
      bias0[r] = asf(opq(((d0 & 1) == 0 && d0 <= 14) ? 0 : (int)asu(NEG)));
      bias1[r] = asf(opq(((d1 & 1) == 0 && d1 >= 0 && d1 <= 14) ? 0 : (int)asu(NEG)));
    }
    struct P1F { bf16x8 xf; bf16x4 w[3][2]; };
    auto f_rd = [&](P1F& f, int u, int i) {
      const int sb = ((u >> 2) % 5) * P1_STAGE, s4 = u & 3;
      if (i == 0) f.xf = ldsb(xa[s4] + sb);
      else f.w[(i - 1) >> 1][(i - 1) & 1] = trr(wa1 + sb + s4 * 3072 + ((i - 1) >> 1) * 64 + ((i - 1) & 1) * 768);
    };
    P1F fa, fb;
    v4u st1;
    auto p1_step = [&](auto uc, P1F& f, P1F& n) {
      constexpr int u = uc.v;
      if constexpr ((u & 3) == 3 && u < 28) { WAITL0(); BAR(); }
      if constexpr (u == 24) ldsc1<0>(st1, rsW, ST1_OFF + (u32)(g * 4096 + (16 + 32 * w + l32) * 16), 0);
      sfor<0, 3>([&](auto ic) {
        constexpr int i = ic.v;
        const bf16x8 wf = cat(f.w[i][0], f.w[i][1]);
        if constexpr (i == 0) aq = mfma32(wf, f.xf, aq);
        if constexpr (i == 1) ak = mfma32(wf, f.xf, ak);
        if constexpr (i == 2) av = mfma32(f.xf, wf, av);
        if constexpr (u < 31) {
          if constexpr (i == 0) { f_rd(n, u + 1, 0); f_rd(n, u + 1, 1); f_rd(n, u + 1, 2); }
          if constexpr (i == 1) { f_rd(n, u + 1, 3); f_rd(n, u + 1, 4); }
          if constexpr (i == 2) { f_rd(n, u + 1, 5); f_rd(n, u + 1, 6); }
        }
        if constexpr (u == 29 && i == 2) {
          asm volatile("s_waitcnt vmcnt(0)" : "+v"(st1), "+v"(xres[0]), "+v"(xres[1]), "+v"(xres[2]), "+v"(xres[3])::"memory");
        }
        SCHED();
      });
    };
    BAR();
    sfor<0, 7>([&](auto i) { f_rd(fa, 0, i.v); });
    sfor<0, 15>([&](auto k) { p1_step(IC<2 * k.v>{}, fa, fb); p1_step(IC<2 * k.v + 1>{}, fb, fa); });
    for (u32 spins = 0; spins < (1u << 20); ++spins) {
      if (__all(st1.y == tag32 && st1.w == tag32)) break;
      __builtin_amdgcn_s_sleep(1);
      ldsc1<0>(st1, rsW, ST1_OFF + (u32)(g * 4096 + (16 + 32 * w + l32) * 16), 0);
      wait_for<0>(st1);
    }
    const float mean = asf(st1.x), rstd = asf(st1.z);
    LAS float* cs_ = lp<float>(M_CS);
    LAS float* mr = lp<float>(M_MR) + w * 64;
    if (lane < 32) { mr[lane] = mean; mr[32 + lane] = rstd; }
    p1_step(IC<30>{}, fa, fb);
    f32x4 mv[4], rv[4];
#pragma unroll
    for (int gg = 0; gg < 4; ++gg) { mv[gg] = *(LAS f32x4*)(mr + 8 * gg + 4 * hh); rv[gg] = *(LAS f32x4*)(mr + 32 + 8 * gg + 4 * hh); }
    p1_step(IC<31>{}, fb, fa);
    STAMP(1);
    BAR();
    f32x4 cq[4], ck[4];
#pragma unroll
    for (int gg = 0; gg < 4; ++gg) { cq[gg] = *(LAS f32x4*)(cs_ + 8 * gg + 4 * hh); ck[gg] = *(LAS f32x4*)(cs_ + 32 + 8 * gg + 4 * hh); }
    const float csv = cs_[64 + l32];
    SCHED();
    v2u qp_[2][2], kp[2][2], vp[2][2];
    {
      u32 pq[8], pk[8], pv[8];
#pragma unroll
      for (int gg = 0; gg < 4; ++gg) {
        float a[4], k4[4], v4[4];
#pragma unroll
        for (int r = 0; r < 4; ++r) {
          a[r] = __builtin_fmaf(-mean, cq[gg][r], aq[4 * gg + r]);
          k4[r] = rstd * __builtin_fmaf(-mean, ck[gg][r], ak[4 * gg + r]);
          v4[r] = rv[gg][r] * __builtin_fmaf(-mv[gg][r], csv, av[4 * gg + r]);
        }
        pq[2 * gg] = pack2(a[0], a[1]); pq[2 * gg + 1] = pack2(a[2], a[3]);
        pk[2 * gg] = pack2(k4[0], k4[1]); pk[2 * gg + 1] = pack2(k4[2], k4[3]);
        pv[2 * gg] = pack2(v4[0], v4[1]); pv[2 * gg + 1] = pack2(v4[2], v4[3]);
      }
#pragma unroll
      for (int x = 0; x < 2; ++x) {
#pragma unroll
        for (int d = 0; d < 2; ++d) {
          const auto sq_ = __builtin_amdgcn_permlane16_swap(pq[4 * x + d], pq[4 * x + 2 + d], false, false);
          qp_[0][x][d] = sq_[0]; qp_[1][x][d] = sq_[1];
          const auto sk_ = __builtin_amdgcn_permlane16_swap(pk[4 * x + d], pk[4 * x + 2 + d], false, false);
          kp[0][x][d] = sk_[0]; kp[1][x][d] = sk_[1];
          const auto s1 = __builtin_amdgcn_permlane32_swap(pv[4 * x + d], pv[4 * x + 2 + d], false, false);
          const auto s2 = __builtin_amdgcn_permlane16_swap(s1[0], s1[1], false, false);
          vp[x][0][d] = s2[0]; vp[x][1][d] = s2[1];
        }
      }
    }
    {
#pragma unroll
      for (int hd = 0; hd < 2; ++hd) {
        *lp<v2u>(KV_OFF + w * 2048 + hd * 512 + lane * 8) = kp[1][hd];
        *lp<v2u>(KV_OFF + w * 2048 + (2 + hd) * 512 + lane * 8) = vp[1][hd];
      }
    }
    WAITL0();
    BAR();
    v2u kprev[2], vprev[2];
    {
      const int pb = w == 0 ? KVH_OFF : KV_OFF + (w - 1) * 2048;
#pragma unroll
      for (int hd = 0; hd < 2; ++hd) {
        kprev[hd] = *lp<v2u>(pb + hd * 512 + lane * 8);
        vprev[hd] = *lp<v2u>(pb + (2 + hd) * 512 + lane * 8);
      }
    }
    const auto rq = __builtin_amdgcn_permlane16_swap(asu(rstd), asu(rstd), false, false);
    const v2u ones2 = {0x3F803F80u, 0x3F803F80u};
    auto attend = [&](int t, int hd, v2u kA, v2u vA, bool prev_ok) {
      const float sc = SC * asf(rq[t]);
      f32x4 s0 = mfma16k16(kA, qp_[t][hd], f32x4{});
      f32x4 s1 = mfma16k16(kp[t][hd], qp_[t][hd], f32x4{});
      float e[8];
#pragma unroll
      for (int r = 0; r < 4; ++r) {
        e[r] = __builtin_fmaf(s0[r], sc, prev_ok ? bias0[r] : NEG);
        e[4 + r] = __builtin_fmaf(s1[r], sc, bias1[r]);
      }
      float mx = fmaxf(fmaxf(fmaxf(e[0], e[1]), fmaxf(e[2], e[3])), fmaxf(fmaxf(e[4], e[5]), fmaxf(e[6], e[7])));
      mx = xmax32(xmax16(mx));
#pragma unroll
      for (int r = 0; r < 8; ++r) e[r] = __builtin_amdgcn_exp2f(e[r] - mx);
      const v2u p0 = {pack2(e[0], e[1]), pack2(e[2], e[3])}, p1 = {pack2(e[4], e[5]), pack2(e[6], e[7])};
      f32x4 sm = mfma16k16(ones2, p0, f32x4{});
      sm = mfma16k16(ones2, p1, sm);
      f32x4 o = mfma16k16(vA, p0, f32x4{});
      o = mfma16k16(vp[t][hd], p1, o);
      const float inv = __builtin_amdgcn_rcpf(sm[0]);
      return v2u{pack2(o[0] * inv, o[1] * inv), pack2(o[2] * inv, o[3] * inv)};
    };
    {
      const bool ok = T0 + 32 * w > 0;
      const v2u o10 = attend(1, 0, kp[0][0], vp[0][0], true), o11 = attend(1, 1, kp[0][1], vp[0][1], true);
      const v2u o00 = attend(0, 0, kprev[0], vprev[0], ok), o01 = attend(0, 1, kprev[1], vprev[1], ok);
      stsc1(rsW, O_OFF + (u32)(((g * 8 + 2 * w + 1) * 16 + m) * 1024 + lane * 16), v4u{o10.x, o10.y, o11.x, o11.y});
      stsc1(rsW, O_OFF + (u32)(((g * 8 + 2 * w) * 16 + m) * 1024 + lane * 16), v4u{o00.x, o00.y, o01.x, o01.y});
    }
  }
  STAMP(2);
  WAITVN(0);
  publish(fl, m, tag);
  STAMP(3);

  {
    bf16x8 w2[8][4];
    int a[4];
    {
      const int row = 4 * q + qp, f = ((row >> 1) & 3) << 1;
#pragma unroll
      for (int n = 0; n < 4; ++n) a[n] = opq(W2_OFF + row * 128 + (((2 * n + (pp >> 1)) ^ f) * 16) + (pp & 1) * 8);
#pragma unroll
      for (int s = 0; s < 8; ++s)
#pragma unroll
        for (int n = 0; n < 4; ++n) w2[s][n] = tr2(a[n] + s * 4096, 2048);
    }
    poll(fl, m, 16, tag);
    BAR();
    STAMP(4);
    v4u of[16];
    const u32 ob = O_OFF + (u32)((g * 8 + 4 * th + w) * 16) * 1024;
    sfor<0, 16>([&](auto i) { ldsc1<0>(of[i.v], rsW, lane * 16, ob + (u32)((i.v + 2 * ct) & 15) * 1024); });
    f32x4 acc[4] = {};
    sfor<0, 16>([&](auto sc) {
      constexpr int s = sc.v;
      wait_for<15 - s>(of[s]);
      const bf16x8 bfr = __builtin_bit_cast(bf16x8, of[s]);
#pragma unroll
      for (int n = 0; n < 4; ++n) {
        acc[n] = mfma16(w2[s & 7][n], bfr, acc[n]);
        if constexpr (s < 8) w2[s][n] = tr2(a[n] + (s + 8) * 4096, 2048);
      }
      SCHED();
    });
    STAMP(5);
    float s = 0.f, ss = 0.f;
    const int tl = 16 * w + tok;
    const int t32 = g * 4 + 2 * th + (w >> 1);
    const u32 hoff = H_OFF + (u32)((t32 * 32 + 4 * ct) * 1024 + (16 * (w & 1) + tok + 32 * (q >> 1)) * 16 + (q & 1) * 8);
#pragma unroll
    for (int n = 0; n < 4; ++n) {
      const u32 h01 = pack2(bflo(xres[n].x) + acc[n][0], bfhi(xres[n].x) + acc[n][1]);
      const u32 h23 = pack2(bflo(xres[n].y) + acc[n][2], bfhi(xres[n].y) + acc[n][3]);
      const v2u hp = {h01, h23};
      stsc1_8(rsW, hoff + n * 1024, hp);
      *lp<v2u>(M_HK + tl * HK_PITCH + (16 * n + 4 * q) * 2) = hp;
      const float a0 = bflo(h01), a1 = bfhi(h01), a2 = bflo(h23), a3 = bfhi(h23);
      s += (a0 + a1) + (a2 + a3);
      ss = __builtin_fmaf(a0, a0, ss); ss = __builtin_fmaf(a1, a1, ss); ss = __builtin_fmaf(a2, a2, ss); ss = __builtin_fmaf(a3, a3, ss);
    }
    s = xadd32(xadd16(s)); ss = xadd32(xadd16(ss));
    if (q == 0) stsc1_8(rsW, ST_OFF + (u32)((ct * S + T0 + 64 * th + tl) * 8), v2u{asu(s), asu(ss)});
  }
  WAITVN(0);
  WAITL0();
  publish(fl + 256, m, tag);
  STAMP(6);

  {
    const int wa = (8 * hh + qp) * 192 + (q & 1) * 32 + pp * 8;
    bf16x8 wA[6], wB[6];
    auto w3_read1 = [&](bf16x8& dst, int base, int i) { const int o = base + (i & 1) * 12288 + (i >> 1) * 64; dst = tr2(o, 768); };
    poll(fl + 256, m, 16, tag);
    BAR();
    STAMP(7);
    const int t32 = g * 4 + w;
    v2u st[4];
#pragma unroll
    for (int i = 0; i < 4; ++i) ldsc1_8(st[i], rsW, (u32)(((hh * 4 + i) * S + l32) * 8), ST_OFF + (u32)(t32 * 32) * 8);
    v4u hf[16];
    const u32 hb = H_OFF + (u32)(t32 * 32) * 1024;
    auto ldh = [&](auto i) { ldsc1<(i.v & 3) * 1024>(hf[i.v & 15], rsW, lane * 16, hb + (u32)(((i.v >> 2) + m) & 7) * 4096); };
    sfor<0, 16>(ldh);
#pragma unroll
    for (int i = 0; i < 6; ++i) w3_read1(wA[i], wa, i);
    asm volatile("s_waitcnt vmcnt(16)" : "+v"(st[0]), "+v"(st[1]), "+v"(st[2]), "+v"(st[3])::"memory");
    float rs, nb;
    {
      float s = (asf(st[0].x) + asf(st[1].x)) + (asf(st[2].x) + asf(st[3].x)), ss = (asf(st[0].y) + asf(st[1].y)) + (asf(st[2].y) + asf(st[3].y));
      s = xadd32(s); ss = xadd32(ss);
      const float mu = s * (1.0f / D);
      rs = __builtin_amdgcn_rsqf(fmaxf(__builtin_fmaf(-mu, mu, ss * (1.0f / D)), 0.f) + 1e-5f);
      nb = -mu * rs;
    }
    auto ln = [&](v4u h, int d) {
      const f32x2 v = {bflo(h[d]), bfhi(h[d])};
      const f32x2 r = __builtin_elementwise_fma(v, f32x2{rs, rs}, f32x2{nb, nb});
      return pack2(r[0], r[1]);
    };
    v4u yA, yB;
    wait_for<15>(hf[0]);
#pragma unroll
    for (int d = 0; d < 4; ++d) yA[d] = ln(hf[0], d);
    ldh(IC<16>{});
    f32x16 acc[6] = {};
    auto step = [&](auto sc, bf16x8(&wc)[6], bf16x8(&wn)[6], v4u& yc, v4u& yn) {
      constexpr int s = sc.v, c1 = (s + 1) >> 2;
      if constexpr (s + 1 < 32) wait_for<(16 + s < 31 ? 16 + s : 31) - (s + 1)>(hf[(s + 1) & 15]);
      if constexpr (s >= 4 && (s & 3) == 0) BAR();
      const int base = opq(wa + (c1 % 6) * SLOT + (16 * ((s + 1) & 3)) * 192);
      const bf16x8 yb = __builtin_bit_cast(bf16x8, yc);
      __builtin_amdgcn_s_waitcnt(0xC07F);
      sfor<0, 6>([&](auto ic) {
        constexpr int i = ic.v;
        acc[i] = mfma32(wc[i], yb, acc[i]);
        if constexpr (s + 1 < 32) {
          if constexpr (i < 3) { w3_read1(wn[2 * i], base, 2 * i); w3_read1(wn[2 * i + 1], base, 2 * i + 1); }
          if constexpr (i >= 2) yn[i - 2] = ln(hf[(s + 1) & 15], i - 2);
        }
        if constexpr (i == 5 && 17 + s < 32) ldh(IC<(17 + s) & 31>{});
        SCHED();
      });
    };
    sfor<0, 14>([&](auto k) { step(IC<2 * k.v>{}, wA, wB, yA, yB); step(IC<2 * k.v + 1>{}, wB, wA, yB, yA); });
    {
      BAR();
      v4u y4[4];
      y4[0] = yA;
      sfor<1, 4>([&](auto j) {
        wait_for<3 - j.v>(hf[(28 + j.v) & 15]);
#pragma unroll
        for (int d = 0; d < 4; ++d) y4[j.v][d] = ln(hf[(28 + j.v) & 15], d);
      });
      const int base = opq(wa + (7 % 6) * SLOT);
      auto epi = [&](int i, int s) {
        v4u o;
#pragma unroll
        for (int d = 0; d < 4; ++d) {
          const int r = 8 * s + 2 * d;
          o[d] = pack2(fmaxf(acc[2 * i][r], 0.f) * acc[2 * i + 1][r], fmaxf(acc[2 * i][r + 1], 0.f) * acc[2 * i + 1][r + 1]);
        }
        const int f0 = 96 * m + 32 * i;
        if (f0 < F) stsc1(rsW, A_OFF + (u32)((t32 * 86 + f0 / 16 + s) * 1024 + lane * 16), o);
      };
      bf16x8 wl[3][8];
      auto rd_pair = [&](int i) {
#pragma unroll
        for (int j = 0; j < 4; ++j) { w3_read1(wl[i][2 * j], base + 16 * j * 192, 2 * i); w3_read1(wl[i][2 * j + 1], base + 16 * j * 192, 2 * i + 1); }
      };
      rd_pair(0); rd_pair(1);
      SCHED();
      sfor<0, 3>([&](auto ic) {
        constexpr int i = ic.v;
        if constexpr (i == 0) rd_pair(2);
        sfor<0, 4>([&](auto jc) {
          constexpr int j = jc.v;
          const bf16x8 yb = __builtin_bit_cast(bf16x8, y4[j]);
          acc[2 * i] = mfma32(wl[i][2 * j], yb, acc[2 * i]);
          acc[2 * i + 1] = mfma32(wl[i][2 * j + 1], yb, acc[2 * i + 1]);
          if constexpr (i > 0 && j == 1) epi(i - 1, 0);
          if constexpr (i > 0 && j == 3) epi(i - 1, 1);
          SCHED();
        });
      });
      STAMP(8);
      BAR();
      epi(2, 0); epi(2, 1);
    }
    WAITVN(0);
  }
  publish(fl + 512, m, tag);
  STAMP(9);

  {
    poll(fl + 512, m, 15, tag);
    BAR();
    STAMP(10);
    const int mh = w & 1, kh = w >> 1;
    const int t32 = g * 4 + 2 * th + mh;
    v4u af[36];
    const u32 ab = A_OFF + (u32)(t32 * 86 + kh * 43) * 1024;
    auto lda = [&](auto i) { const int ps = i.v + 5 * ct; ldsc1<0>(af[i.v % 36], rsW, lane * 16, ab + (u32)(ps >= 43 ? ps - 43 : ps) * 1024); };
    sfor<0, 14>(lda);
    f32x16 acc[2] = {};
    int wa[2];
#pragma unroll
    for (int n = 0; n < 2; ++n) wa[n] = (4 * hh + qp) * 128 + ((n ^ ((qp >> 1) & 1)) * 64) + (q & 1) * 32 + pp * 8;
    bf16x8 wA[2], wB[2];
    auto w4_read = [&](bf16x8(&dst)[2], auto sc) {
      constexpr int s = sc.v;
      const int off = s < 42 ? ((s / 6 + 2) % 6) * SLOT + kh * 12288 + (s % 6) * 2048 : M_W4T + kh * 2048;
#pragma unroll
      for (int n = 0; n < 2; ++n) dst[n] = tr2(opq(wa[n] + off), 1024);
    };
    w4_read(wA, IC<0>{});
    auto step = [&](auto sc, bf16x8(&wc)[2], bf16x8(&wn)[2]) {
      constexpr int s = sc.v;
      constexpr int issued = (14 + 2 * (s < 8 ? s : 8) + (s > 8 ? s - 8 : 0)) < 43 ? (14 + 2 * (s < 8 ? s : 8) + (s > 8 ? s - 8 : 0)) : 43;
      wait_for<issued - 1 - s>(af[s % 36]);
      if constexpr (s == 6 || s == 30) BAR();
      if constexpr (s + 1 < 43) w4_read(wn, IC<s + 1>{});
      const bf16x8 bfr = __builtin_bit_cast(bf16x8, af[s % 36]);
      acc[0] = mfma32(wc[0], bfr, acc[0]);
      if constexpr (issued < 43) lda(IC<(issued < 43 ? issued : 0)>{});
      SCHED();
      acc[1] = mfma32(wc[1], bfr, acc[1]);
      if constexpr (s < 8 && issued + 1 < 43) lda(IC<(issued + 1 < 43 ? issued + 1 : 0)>{});
      SCHED();
    };
    sfor<0, 21>([&](auto k) { step(IC<2 * k.v>{}, wA, wB); step(IC<2 * k.v + 1>{}, wB, wA); });
    step(IC<42>{}, wA, wB);
    STAMP(11);
    LAS f32x4* red = lp<f32x4>(3 * SLOT);
    {
      const f32x16 snd = kh ? acc[0] : acc[1];
#pragma unroll
      for (int gg = 0; gg < 4; ++gg) red[(w * 4 + gg) * 64 + lane] = f32x4{snd[4 * gg], snd[4 * gg + 1], snd[4 * gg + 2], snd[4 * gg + 3]};
    }
    if (m == 0 && tid == 0) __hip_atomic_store(ep, tag, RLX_AGENT);
    WAITL0(); BAR();
    {
      const f32x16 keep = kh ? acc[1] : acc[0];
      const int pw = w ^ 2;
      GAS u16* orow = (GAS u16*)p.out + (size_t)(T0 + 64 * th + 32 * mh + l32) * D + 64 * ct + 32 * kh + 4 * hh;
#pragma unroll
      for (int gg = 0; gg < 4; ++gg) {
        const f32x4 o = red[(pw * 4 + gg) * 64 + lane];
        const v2u hk = *lp<v2u>(M_HK + (32 * mh + l32) * HK_PITCH + (32 * kh + 8 * gg + 4 * hh) * 2);
        v2u r;
        r.x = pack2(bflo(hk.x) + (keep[4 * gg] + o[0]), bfhi(hk.x) + (keep[4 * gg + 1] + o[1]));
        r.y = pack2(bflo(hk.y) + (keep[4 * gg + 2] + o[2]), bfhi(hk.y) + (keep[4 * gg + 3] + o[3]));
        *(GAS v2u*)(orow + 8 * gg) = r;
      }
    }
  }
  STAMP(12);
}

extern "C" void kernel_launch(void* const* d_in, const int* in_sizes, int n_in, void* d_out, int out_size,
                              void* d_ws, size_t ws_size, hipStream_t stream) {
  Params p;
  p.x = (const u16*)d_in[0]; p.wqkv = (const u16*)d_in[1]; p.wo = (const u16*)d_in[2];
  p.wg = (const u16*)d_in[4]; p.wu = (const u16*)d_in[5]; p.wd = (const u16*)d_in[6];
  p.out = (u16*)d_out; p.ws = (unsigned char*)d_ws;
  (void)hipFuncSetAttribute((const void*)fused_block, hipFuncAttributeMaxDynamicSharedMemorySize, LDS_BYTES);
  fused_block<<<256, 512, LDS_BYTES, stream>>>(p);
}
```

```cpp
#include <hip/hip_runtime.h>
#include <stdint.h>

typedef unsigned short u16;
typedef unsigned u32;
typedef unsigned long long u64;
typedef short bf16x8 __attribute__((ext_vector_type(8)));
typedef short bf16x4 __attribute__((ext_vector_type(4)));
typedef float f32x2 __attribute__((ext_vector_type(2)));
typedef float f32x4 __attribute__((ext_vector_type(4)));
typedef float f32x16 __attribute__((ext_vector_type(16)));
typedef unsigned v4u __attribute__((ext_vector_type(4)));
typedef unsigned v2u __attribute__((ext_vector_type(2)));
typedef __bf16 bf2_t __attribute__((ext_vector_type(2)));
#define GAS __attribute__((address_space(1)))
#define LAS __attribute__((address_space(3)))
#define DI __device__ __forceinline__
#define RLX_AGENT __ATOMIC_RELAXED, __HIP_MEMORY_SCOPE_AGENT
typedef GAS u64 gu64;
typedef __amdgpu_buffer_rsrc_t rsrc_t;

constexpr int S = 2048, D = 512, F = 1376;
constexpr u32 O_OFF = 0;
constexpr u32 H_OFF = 2u << 20;
constexpr u32 A_OFF = 4u << 20;
constexpr u32 ST_OFF = 10u << 20;
constexpr u32 ST1_OFF = (10u << 20) + (512u << 10);
constexpr u32 FL_OFF = 11u << 20;
constexpr u32 EP_OFF = 12u << 20;
constexpr u32 WS_BYTES = 13u << 20;

constexpr int SLOT = 24576;
constexpr int MISC = 6 * SLOT;
constexpr int LDS_BYTES = 163840;
constexpr int P1_STAGE = 30720;
constexpr int P1_WOFF = 18432;
constexpr int W2_OFF = 3 * P1_STAGE;
constexpr int M_CS = 157696;
constexpr int M_MR = M_CS + 1024;
constexpr int KVH_OFF = M_CS + 2048;
constexpr int KV_OFF = 3 * SLOT;
constexpr int M_HK = MISC;
constexpr int M_W4T = MISC + 8704;
constexpr int HK_PITCH = 136;

struct Params {
  const u16* x; const u16* wqkv; const u16* wo; const u16* wg; const u16* wu; const u16* wd;
  u16* out; unsigned char* ws;
};

template <int I> struct IC { static constexpr int v = I; };
template <int I, int N, class Fn> DI void sfor(Fn&& f) { if constexpr (I < N) { f(IC<I>{}); sfor<I + 1, N>(f); } }

DI u32 pack2(float a, float b) { f32x2 v = {a, b}; bf2_t r = __builtin_convertvector(v, bf2_t); return __builtin_bit_cast(u32, r); }
DI float bflo(u32 u) { return __builtin_bit_cast(float, u << 16); }
DI float bfhi(u32 u) { return __builtin_bit_cast(float, u & 0xffff0000u); }
DI float asf(u32 u) { return __builtin_bit_cast(float, u); }
DI u32 asu(float f) { return __builtin_bit_cast(u32, f); }
DI f32x4 mfma16(bf16x8 a, bf16x8 b, f32x4 c) { return __builtin_amdgcn_mfma_f32_16x16x32_bf16(a, b, c, 0, 0, 0); }
DI f32x4 mfma16k16(v2u a, v2u b, f32x4 c) { return __builtin_amdgcn_mfma_f32_16x16x16bf16_1k(__builtin_bit_cast(bf16x4, a), __builtin_bit_cast(bf16x4, b), c, 0, 0, 0); }
DI f32x16 mfma32(bf16x8 a, bf16x8 b, f32x16 c) { return __builtin_amdgcn_mfma_f32_32x32x16_bf16(a, b, c, 0, 0, 0); }
DI bf16x8 cat(bf16x4 a, bf16x4 b) { return __builtin_shufflevector(a, b, 0, 1, 2, 3, 4, 5, 6, 7); }
#pragma clang diagnostic ignored "-Wint-to-pointer-cast"
DI bf16x4 trr(int off) { return __builtin_amdgcn_ds_read_tr16_b64_v4i16((LAS bf16x4*)(u32)off); }
DI bf16x8 tr2(int off, int d) { return cat(trr(off), trr(off + d)); }
DI bf16x8 ldsb(int off) { return *(LAS bf16x8*)(u32)off; }
template <class T> DI LAS T* lp(int off) { return (LAS T*)(u32)off; }
DI rsrc_t mkrs(const void* p, u32 bytes) { return __builtin_amdgcn_make_buffer_rsrc((void*)p, 0, bytes, 0x00020000); }
DI u32 rfl(u32 v) { return __builtin_amdgcn_readfirstlane(v); }
DI int opq(int v) { asm volatile("" : "+v"(v)); return v; }
DI float xmax16(float v) { auto r = __builtin_amdgcn_permlane16_swap(asu(v), asu(v), false, false); return fmaxf(asf(r[0]), asf(r[1])); }
DI float xmax32(float v) { auto r = __builtin_amdgcn_permlane32_swap(asu(v), asu(v), false, false); return fmaxf(asf(r[0]), asf(r[1])); }
DI float xadd16(float v) { auto r = __builtin_amdgcn_permlane16_swap(asu(v), asu(v), false, false); return asf(r[0]) + asf(r[1]); }
DI float xadd32(float v) { auto r = __builtin_amdgcn_permlane32_swap(asu(v), asu(v), false, false); return asf(r[0]) + asf(r[1]); }
#define BAR() __builtin_amdgcn_s_barrier()
#define SCHED() __builtin_amdgcn_sched_barrier(0)
#define WAITVN(n) asm volatile("s_waitcnt vmcnt(%0)" ::"i"(n) : "memory")
#define WAITL0() asm volatile("s_waitcnt lgkmcnt(0)" ::: "memory")

DI void dma(rsrc_t rs, u32 voff, u32 soff, u32 ldsaddr) {
  asm volatile("s_mov_b32 m0, %0\n\ts_nop 0\n\tbuffer_load_dwordx4 %1, %2, %3 offen lds" ::"s"(ldsaddr), "v"(voff), "s"(rs), "s"(soff) : "memory");
}
template <int IMM> DI void ldsc1(v4u& dst, rsrc_t rs, u32 voff, u32 soff) {
  asm volatile("buffer_load_dwordx4 %0, %1, %2, %3 offen offset:%4 sc1" : "=v"(dst) : "v"(voff), "s"(rs), "s"(soff), "i"(IMM) : "memory");
}
DI void ldsc1_8(v2u& dst, rsrc_t rs, u32 voff, u32 soff) {
  asm volatile("buffer_load_dwordx2 %0, %1, %2, %3 offen sc1" : "=v"(dst) : "v"(voff), "s"(rs), "s"(soff) : "memory");
}
DI void ld16(v4u& dst, rsrc_t rs, u32 voff, u32 soff) {
  asm volatile("buffer_load_dwordx4 %0, %1, %2, %3 offen" : "=v"(dst) : "v"(voff), "s"(rs), "s"(soff) : "memory");
}
DI void ld4_keep(u32& dst, rsrc_t rs, u32 voff) { asm volatile("buffer_load_dword %0, %1, %2, 0 offen" : "+v"(dst) : "v"(voff), "s"(rs) : "memory"); }
template <int IMM> DI void ld8(v2u& dst, rsrc_t rs, u32 voff, u32 soff) {
  asm volatile("buffer_load_dwordx2 %0, %1, %2, %3 offen offset:%4" : "=v"(dst) : "v"(voff), "s"(rs), "s"(soff), "i"(IMM) : "memory");
}
template <int N> DI void wait_for(v4u& a) { asm volatile("s_waitcnt vmcnt(%1)" : "+v"(a) : "i"(N > 63 ? 63 : N) : "memory"); }
DI void stsc1(rsrc_t rs, u32 off, v4u v) { __builtin_amdgcn_raw_buffer_store_b128(v, rs, off, 0, 16); }
DI void stsc1_8(rsrc_t rs, u32 off, v2u v) { __builtin_amdgcn_raw_buffer_store_b64(v, rs, off, 0, 16); }

#define STAMP(i) do {} while (0)

DI void publish(gu64* fl, int m, u64 tag) {
  BAR();
  if (threadIdx.x < 16) __hip_atomic_store(fl + threadIdx.x * 16 + m, tag, RLX_AGENT);
}
DI void poll(gu64* fl, int m, int nprod, u64 tag) {
  if (threadIdx.x < 64) {
    const int lane = threadIdx.x;
    for (u32 spins = 0; spins < (1u << 20); ++spins) {
      u64 v = tag;
      if (lane < nprod) v = __hip_atomic_load(fl + m * 16 + lane, RLX_AGENT);
      if (__all(v == tag)) break;
    }
    __builtin_amdgcn_fence(__ATOMIC_ACQUIRE, "wavefront");
  }
}

__global__ __launch_bounds__(512, 2) void fused_block(Params p) {
  extern __shared__ __attribute__((aligned(16))) unsigned char smem[];
  const int tid = threadIdx.x, lane = tid & 63, w8 = (int)rfl(tid >> 6), w = w8 & 3;
  const int b = blockIdx.x, g = (b & 7) * 2 + ((b >> 3) >> 4), m = (b >> 3) & 15;
  const int T0 = g * 128;
  const int tok = lane & 15, q = lane >> 4;
  const int th = m >> 3, ct = m & 7;
  const rsrc_t rsW = mkrs(p.ws, WS_BYTES);
  const rsrc_t rsX = mkrs(p.x, S * D * 2);
  const u32 halo0 = (u32)(T0 >= 16 ? T0 - 16 : 0);
  asm volatile("" ::"s"((u32)(size_t)smem));

  if (w8 >= 4) {
    const rsrc_t rsW1 = mkrs(p.wqkv, D * 3 * D * 2);
    v2u epv;
    ldsc1_8(epv, rsW, 0, EP_OFF + g * 128);
    u32 vx0, vx1, vxh, vw0, vw1, vw2;
    {
      const int r8 = lane >> 3, sl = lane & 7;
      vx0 = r8 * 1024 + ((sl ^ ((lane >> 4) & 7)) * 16);
      vx1 = r8 * 1024 + ((sl ^ ((4 + (lane >> 4)) & 7)) * 16);
      vxh = (w & 1) ? vx1 : vx0;
      auto pat = [&](int j) { const int e = j * 64 + lane, r = e / 12, pc = e % 12; return (u32)(r * 3072 + (pc >> 2) * 1024 + (pc & 3) * 16); };
      vw0 = pat(0); vw1 = pat(1); vw2 = pat(2);
    }
    const u32 sxh = (halo0 + 8 * (w & 1)) * 1024, sxm = (u32)(T0 + 32 * w) * 1024, sw1 = (u32)(16 * w) * 3072 + m * 64;
    const int rot = (g + m) & 7;
    auto p1_piece = [&](int cc, int j) {
      const u32 lb = (cc % 5) * P1_STAGE;
      const u32 c = (u32)((cc + rot) & 7);
      if (j == 0) dma(rsX, vxh, sxh + c * 128, lb + (w & 1) * 1024);
      if (j == 1) dma(rsW1, vw0, sw1 + c * (64 * 3072), lb + P1_WOFF + (3 * w) * 1024);
      if (j == 2) dma(rsX, vx0, sxm + c * 128, lb + (2 + 4 * w) * 1024);
      if (j == 3) dma(rsW1, vw1, sw1 + c * (64 * 3072), lb + P1_WOFF + (3 * w + 1) * 1024);
      if (j == 4) dma(rsX, vx1, sxm + c * 128 + 8192, lb + (3 + 4 * w) * 1024);
      if (j == 5) dma(rsW1, vw2, sw1 + c * (64 * 3072), lb + P1_WOFF + (3 * w + 2) * 1024);
      if (j == 6) dma(rsX, vx0, sxm + c * 128 + 16384, lb + (4 + 4 * w) * 1024);
      if (j == 7) dma(rsX, vx1, sxm + c * 128 + 24576, lb + (5 + 4 * w) * 1024);
    };
    const rsrc_t rsW2 = mkrs(p.wo, D * D * 2), rsG = mkrs(p.wg, D * F * 2), rsU = mkrs(p.wu, D * F * 2), rsD = mkrs(p.wd, F * D * 2);
    const u32 v2 = (lane >> 3) * 1024 + (((lane & 7) ^ (((lane >> 4) & 3) << 1)) * 16);
    auto w2_piece = [&](int i) {
      const int ii = 16 * w + i, ls = ii >> 2;
      dma(rsW2, v2, (u32)(32 * ((ls + 2 * ct) & 15) + 8 * (ii & 3)) * 1024 + ct * 128, W2_OFF + ii * 1024);
    };
    const int qp = (lane & 15) >> 2, pp = lane & 3;
    f32x4 ahA = {}, ahB = {};
    const int xha0 = tok * 128 + ((q ^ (tok >> 1)) * 16), xha1 = tok * 128 + (((4 + q) ^ (tok >> 1)) * 16);
    const int wha = P1_WOFF + (8 * q + qp) * 192 + (1 + (w >> 1)) * 64 + (w & 1) * 32 + pp * 8;
    const int whb = P1_WOFF + (8 * q + qp) * 192 + (w < 2 ? 0 : 128) + (w & 1) * 32 + pp * 8;
    auto halo_with = [&](int hc, auto&& issue) {
      const int sb = (hc % 5) * P1_STAGE;
      v4u xh[2]; bf16x8 fa[2], fb[2];
#pragma unroll
      for (int s2 = 0; s2 < 2; ++s2) {
        xh[s2] = __builtin_bit_cast(v4u, ldsb((s2 ? xha1 : xha0) + sb));
        fa[s2] = tr2(wha + sb + s2 * 6144, 768); fb[s2] = tr2(whb + sb + s2 * 6144, 768);
      }
      SCHED();
      issue(0); issue(1); issue(2); issue(3);
      SCHED();
#pragma unroll
      for (int s2 = 0; s2 < 2; ++s2) {
#pragma unroll
        for (int d = 0; d < 4; ++d) xh[s2][d] = tok == 0 ? 0x3F803F80u : xh[s2][d];
        const bf16x8 xhb = __builtin_bit_cast(bf16x8, xh[s2]);
        ahA = mfma16(fa[s2], xhb, ahA);
        ahB = mfma16(fb[s2], xhb, ahB);
      }
      SCHED();
      issue(4); issue(5); issue(6); issue(7);
    };
    sfor<0, 24>([&](auto i) { p1_piece(i.v >> 3, i.v & 7); });
    WAITVN(16); BAR();
    halo_with(0, [&](int j) { p1_piece(3, j); });
    WAITVN(16); BAR();
    halo_with(1, [&](int j) { p1_piece(4, j); });
    WAITVN(16); BAR();
    halo_with(2, [&](int j) { p1_piece(5, j); });
    WAITVN(16); BAR();
    halo_with(3, [&](int j) { p1_piece(6, j); });
    WAITVN(16); BAR();
    halo_with(4, [&](int j) { p1_piece(7, j); });
    u32 touch = 0;
    {
      const u32 wg_ = (u32)(b * 4 + w);
      const u32 o4 = (u32)lane < 4u ? (wg_ * 4u + lane) * 128u : 0xFFFFFF00u, o11 = (u32)lane < 11u ? (wg_ * 11u + lane) * 128u : 0xFFFFFF00u;
      ld4_keep(touch, rsW2, o4); ld4_keep(touch, rsG, o11); ld4_keep(touch, rsU, o11); ld4_keep(touch, rsD, o11);
    }
    v4u sth;
    ldsc1<0>(sth, rsW, ST1_OFF + (u32)(g * 4096 + tok * 16), 0);
    WAITVN(21); BAR();
    halo_with(5, [&](int) {});
    WAITVN(13); BAR();
    halo_with(6, [&](int) {});
    WAITVN(5); BAR();
    halo_with(7, [&](int j) { w2_piece(j); });
    asm volatile("s_waitcnt vmcnt(8)" : "+v"(sth), "+v"(touch), "+v"(epv)::"memory");
    const u64 tag = (((u64)rfl(epv.y) << 32) | rfl(epv.x)) + 1;
    const u32 tag32 = (u32)tag ^ (u32)(tag >> 32) ^ 0x9E3779B9u;
    for (u32 spins = 0; spins < (1u << 20); ++spins) {
      if (__all(sth.y == tag32 && sth.w == tag32)) break;
      __builtin_amdgcn_s_sleep(1);
      ldsc1<0>(sth, rsW, ST1_OFF + (u32)(g * 4096 + tok * 16), 0);
      wait_for<0>(sth);
    }
    {
      LAS float* cs_ = lp<float>(M_CS);
      if (tok == 0) {
        *(LAS f32x4*)(cs_ + 32 + 16 * w + 4 * q) = ahA;
        if (w < 2) *(LAS f32x4*)(cs_ + 16 * w + 4 * q) = ahB;
      }
      const float hmu = asf(sth.x), hrs = asf(sth.z);
      float hv[4];
#pragma unroll
      for (int r = 0; r < 4; ++r) {
        const float c = asf(__builtin_amdgcn_update_dpp(0, asu(ahA[r]), 0x150, 0xf, 0xf, false));
        hv[r] = hrs * __builtin_fmaf(-hmu, c, ahA[r]);
      }
      const u32 o0 = pack2(hv[0], hv[1]), o1 = pack2(hv[2], hv[3]);
      if (w < 2) *lp<v2u>(KVH_OFF + w * 512 + (tok + 16 * (((q & 1) << 1) | (q >> 1))) * 8) = v2u{o0, o1};
      else {
        LAS u16* vv = lp<u16>(KVH_OFF + w * 512) + (4 * q + 16 * (tok >> 2)) * 4 + (tok & 3);
        vv[0] = (u16)o0; vv[4] = (u16)(o0 >> 16); vv[8] = (u16)o1; vv[12] = (u16)(o1 >> 16);
      }
    }
    WAITL0();
    const u32 v30 = (lane / 12) * (F * 2) + (lane % 12) * 16, v31 = ((64 + lane) / 12) * (F * 2) + ((64 + lane) % 12) * 16,
              v32 = ((128 + lane) / 12) * (F * 2) + ((128 + lane) % 12) * 16;
    const u32 s3 = (u32)(16 * w) * (F * 2) + m * 192;
    auto w3_chunk = [&](int c) {
      const u32 so = s3 + (u32)(64 * ((c + m) & 7)) * (F * 2), lb = (c % 6) * SLOT + 3 * w * 1024;
      dma(rsG, v30, so, lb); dma(rsG, v31, so, lb + 1024); dma(rsG, v32, so, lb + 2048);
      dma(rsU, v30, so, lb + 12288); dma(rsU, v31, so, lb + 12288 + 1024); dma(rsU, v32, so, lb + 12288 + 2048);
    };
    const u32 v4s = (lane >> 3) * 1024 + (((lane & 7) ^ (((lane >> 4) & 1) << 2)) * 16);
    auto w4_row = [&](int ls) { const int ps = ls + 5 * ct; return (u32)((ps >= 43 ? ps - 43 : ps) * 16); };
    auto w4_part = [&](int part) {
      const u32 lb = ((part + 2) % 6) * SLOT + 3 * w * 1024;
#pragma unroll
      for (int j = 0; j < 3; ++j) {
        const int jj = 3 * w + j;
        const u32 so = (w4_row(6 * part + (jj >> 1)) + (jj & 1) * 8) * 1024 + ct * 128;
        dma(rsD, v4s, so, lb + j * 1024); dma(rsD, v4s, so + 688 * 1024, lb + 12288 + j * 1024);
      }
    };
    BAR();
    sfor<8, 16>([&](auto i) { w2_piece(i.v); });
    BAR();
    w3_chunk(0); w3_chunk(1);
    WAITVN(12);
    BAR();
    w3_chunk(2);
    BAR();
    BAR();
    w3_chunk(3);
    WAITVN(6); BAR();
    w3_chunk(4); w3_chunk(5);
    BAR(); w3_chunk(6);
    WAITVN(18); BAR(); w3_chunk(7);
    WAITVN(18); BAR(); w4_part(0);
    WAITVN(18); BAR(); w4_part(1);
    WAITVN(18); BAR(); w4_part(2);
    WAITVN(18); BAR(); w4_part(3);
    BAR(); w4_part(4);
    BAR();
    w4_part(5);
    dma(rsD, v4s, ((u32)(w >> 1) * 688 + w4_row(42) + (w & 1) * 8) * 1024 + ct * 128, M_W4T + w * 1024);
    BAR();
    WAITVN(7); BAR();
    WAITVN(0); BAR(); w4_part(6);
    WAITVN(0); BAR();
    BAR();
    return;
  }

  __builtin_amdgcn_s_setprio(2);
  STAMP(0);
  gu64* ep = (gu64*)(p.ws + EP_OFF) + g * 16;
  gu64* fl = (gu64*)(p.ws + FL_OFF) + g * 768;
  v2u epv, xres[4];
  v4u srow[3];
  ldsc1_8(epv, rsW, 0, EP_OFF + g * 128);
  sfor<0, 3>([&](auto jc) {
    const int j = jc.v < 2 ? w + 4 * jc.v : 8, R = 9 * m + j;
    const u32 token = R < 16 ? halo0 + R : (u32)(T0 + R - 16);
    ld16(srow[jc.v], rsX, lane * 16, token * 1024);
  });
  {
    const u32 vo = (u32)tok * 1024 + q * 8, so = (u32)(T0 + 64 * th + 16 * w) * 1024 + ct * 128;
    ld8<0>(xres[0], rsX, vo, so); ld8<32>(xres[1], rsX, vo, so); ld8<64>(xres[2], rsX, vo, so); ld8<96>(xres[3], rsX, vo, so);
  }
  asm volatile("s_waitcnt vmcnt(4)" : "+v"(epv), "+v"(srow[0]), "+v"(srow[1]), "+v"(srow[2])::"memory");
  const u64 tag = (((u64)rfl(epv.y) << 32) | rfl(epv.x)) + 1;
  const u32 tag32 = (u32)tag ^ (u32)(tag >> 32) ^ 0x9E3779B9u;
    {
    float sv[3], qv[3];
#pragma unroll
    for (int j = 0; j < 3; ++j) {
      float s = 0.f, ss = 0.f;
#pragma unroll
      for (int d = 0; d < 4; ++d) {
        const float a = bflo(srow[j][d]), c = bfhi(srow[j][d]);
        s += a + c; ss = __builtin_fmaf(a, a, ss); ss = __builtin_fmaf(c, c, ss);
      }
      sv[j] = s; qv[j] = ss;
    }
    auto s32 = [&](float a, float c) { auto r = __builtin_amdgcn_permlane32_swap(asu(a), asu(c), false, false); return asf(r[0]) + asf(r[1]); };
    auto s16 = [&](float a, float c) { auto r = __builtin_amdgcn_permlane16_swap(asu(a), asu(c), false, false); return asf(r[0]) + asf(r[1]); };
    const float A0 = s32(sv[0], qv[0]), A1 = s32(sv[1], qv[1]), A2 = s32(sv[2], qv[2]);
    float B = s16(A0, A1), C = s16(A2, A2);
    auto ror = [&](float v, auto n) { return v + asf(__builtin_amdgcn_update_dpp(0, asu(v), 0x120 + n.v, 0xf, 0xf, false)); };
    B = ror(B, IC<8>{}); C = ror(C, IC<8>{}); B = ror(B, IC<4>{}); C = ror(C, IC<4>{}); B = ror(B, IC<2>{}); C = ror(C, IC<2>{}); B = ror(B, IC<1>{}); C = ror(C, IC<1>{});
    const auto rb = __builtin_amdgcn_permlane32_swap(asu(B), asu(B), false, false), rc = __builtin_amdgcn_permlane32_swap(asu(C), asu(C), false, false);
    const float s = lane < 32 ? asf(rb[0]) : asf(rc[0]), ss = lane < 32 ? asf(rb[1]) : asf(rc[1]);
    const float mu = s * (1.0f / D), rs = __builtin_amdgcn_rsqf(fmaxf(__builtin_fmaf(-mu, mu, ss * (1.0f / D)), 0.f) + 1e-5f);
    const int j = q < 2 ? w + 4 * q : 8;
    if (tok == 0 && q < 3) stsc1(rsW, ST1_OFF + (u32)(g * 4096 + (9 * m + j) * 16), v4u{asu(mu), tag32, asu(rs), tag32});
  }

  const int l32 = lane & 31, hh = lane >> 5;
  const int qp = (lane & 15) >> 2, pp = lane & 3;
  {
    f32x16 aq = {}, ak = {}, av = {};
    int xa[4], wa1;
#pragma unroll
    for (int s4 = 0; s4 < 4; ++s4) xa[s4] = (16 + 32 * w + l32) * 128 + (((2 * s4 + hh) ^ ((l32 >> 1) & 7)) * 16);
    wa1 = P1_WOFF + (8 * hh + qp) * 192 + (q & 1) * 32 + pp * 8;
    constexpr float SC = 0.25f * 1.4426950408889634f, NEG = -3.0e38f;
    float bias0[4], bias1[4];
#pragma unroll
    for (int r = 0; r < 4; ++r) {
      const int d0 = tok + 16 - (4 * q + r), d1 = tok - (4 * q + r);
      bias0[r] = asf(opq(((d0 & 1) == 0 && d0 <= 14) ? 0 : (int)asu(NEG)));
      bias1[r] = asf(opq(((d1 & 1) == 0 && d1 >= 0 && d1 <= 14) ? 0 : (int)asu(NEG)));
    }
    struct P1F { bf16x8 xf; bf16x4 w[3][2]; };
    auto f_rd = [&](P1F& f, int u, int i) {
      const int sb = ((u >> 2) % 5) * P1_STAGE, s4 = u & 3;
      if (i == 0) f.xf = ldsb(xa[s4] + sb);
      else f.w[(i - 1) >> 1][(i - 1) & 1] = trr(wa1 + sb + s4 * 3072 + ((i - 1) >> 1) * 64 + ((i - 1) & 1) * 768);
    };
    P1F fa, fb;
    v4u st1;
    auto p1_step = [&](auto uc, P1F& f, P1F& n) {
      constexpr int u = uc.v;
      if constexpr ((u & 3) == 3 && u < 28) { WAITL0(); BAR(); }
      if constexpr (u == 24) ldsc1<0>(st1, rsW, ST1_OFF + (u32)(g * 4096 + (16 + 32 * w + l32) * 16), 0);
      sfor<0, 3>([&](auto ic) {
        constexpr int i = ic.v;
        const bf16x8 wf = cat(f.w[i][0], f.w[i][1]);
        if constexpr (i == 0) aq = mfma32(wf, f.xf, aq);
        if constexpr (i == 1) ak = mfma32(wf, f.xf, ak);
        if constexpr (i == 2) av = mfma32(f.xf, wf, av);
        if constexpr (u < 31) {
          if constexpr (i == 0) { f_rd(n, u + 1, 0); f_rd(n, u + 1, 1); f_rd(n, u + 1, 2); }
          if constexpr (i == 1) { f_rd(n, u + 1, 3); f_rd(n, u + 1, 4); }
          if constexpr (i == 2) { f_rd(n, u + 1, 5); f_rd(n, u + 1, 6); }
        }
        if constexpr (u == 29 && i == 2) {
          asm volatile("s_waitcnt vmcnt(0)" : "+v"(st1), "+v"(xres[0]), "+v"(xres[1]), "+v"(xres[2]), "+v"(xres[3])::"memory");
        }
        SCHED();
      });
    };
    BAR();
    sfor<0, 7>([&](auto i) { f_rd(fa, 0, i.v); });
    sfor<0, 15>([&](auto k) { p1_step(IC<2 * k.v>{}, fa, fb); p1_step(IC<2 * k.v + 1>{}, fb, fa); });
    for (u32 spins = 0; spins < (1u << 20); ++spins) {
      if (__all(st1.y == tag32 && st1.w == tag32)) break;
      __builtin_amdgcn_s_sleep(1);
      ldsc1<0>(st1, rsW, ST1_OFF + (u32)(g * 4096 + (16 + 32 * w + l32) * 16), 0);
      wait_for<0>(st1);
    }
    const float mean = asf(st1.x), rstd = asf(st1.z);
    LAS float* cs_ = lp<float>(M_CS);
    LAS float* mr = lp<float>(M_MR) + w * 64;
    if (lane < 32) { mr[lane] = mean; mr[32 + lane] = rstd; }
    p1_step(IC<30>{}, fa, fb);
    f32x4 mv[4], rv[4];
#pragma unroll
    for (int gg = 0; gg < 4; ++gg) { mv[gg] = *(LAS f32x4*)(mr + 8 * gg + 4 * hh); rv[gg] = *(LAS f32x4*)(mr + 32 + 8 * gg + 4 * hh); }
    p1_step(IC<31>{}, fb, fa);
    STAMP(1);
    BAR();
    f32x4 cq[4], ck[4];
#pragma unroll
    for (int gg = 0; gg < 4; ++gg) { cq[gg] = *(LAS f32x4*)(cs_ + 8 * gg + 4 * hh); ck[gg] = *(LAS f32x4*)(cs_ + 32 + 8 * gg + 4 * hh); }
    const float csv = cs_[64 + l32];
    SCHED();
    v2u qp_[2][2], kp[2][2], vp[2][2];
    {
      u32 pq[8], pk[8], pv[8];
#pragma unroll
      for (int gg = 0; gg < 4; ++gg) {
        float a[4], k4[4], v4[4];
#pragma unroll
        for (int r = 0; r < 4; ++r) {
          a[r] = __builtin_fmaf(-mean, cq[gg][r], aq[4 * gg + r]);
          k4[r] = rstd * __builtin_fmaf(-mean, ck[gg][r], ak[4 * gg + r]);
          v4[r] = rv[gg][r] * __builtin_fmaf(-mv[gg][r], csv, av[4 * gg + r]);
        }
        pq[2 * gg] = pack2(a[0], a[1]); pq[2 * gg + 1] = pack2(a[2], a[3]);
        pk[2 * gg] = pack2(k4[0], k4[1]); pk[2 * gg + 1] = pack2(k4[2], k4[3]);
        pv[2 * gg] = pack2(v4[0], v4[1]); pv[2 * gg + 1] = pack2(v4[2], v4[3]);
      }
#pragma unroll
      for (int x = 0; x < 2; ++x) {
#pragma unroll
        for (int d = 0; d < 2; ++d) {
          const auto sq_ = __builtin_amdgcn_permlane16_swap(pq[4 * x + d], pq[4 * x + 2 + d], false, false);
          qp_[0][x][d] = sq_[0]; qp_[1][x][d] = sq_[1];
          const auto sk_ = __builtin_amdgcn_permlane16_swap(pk[4 * x + d], pk[4 * x + 2 + d], false, false);
          kp[0][x][d] = sk_[0]; kp[1][x][d] = sk_[1];
          const auto s1 = __builtin_amdgcn_permlane32_swap(pv[4 * x + d], pv[4 * x + 2 + d], false, false);
          const auto s2 = __builtin_amdgcn_permlane16_swap(s1[0], s1[1], false, false);
          vp[x][0][d] = s2[0]; vp[x][1][d] = s2[1];
        }
      }
    }
    {
#pragma unroll
      for (int hd = 0; hd < 2; ++hd) {
        *lp<v2u>(KV_OFF + w * 2048 + hd * 512 + lane * 8) = kp[1][hd];
        *lp<v2u>(KV_OFF + w * 2048 + (2 + hd) * 512 + lane * 8) = vp[1][hd];
      }
    }
    WAITL0();
    BAR();
    v2u kprev[2], vprev[2];
    {
      const int pb = w == 0 ? KVH_OFF : KV_OFF + (w - 1) * 2048;
#pragma unroll
      for (int hd = 0; hd < 2; ++hd) {
        kprev[hd] = *lp<v2u>(pb + hd * 512 + lane * 8);
        vprev[hd] = *lp<v2u>(pb + (2 + hd) * 512 + lane * 8);
      }
    }
    const auto rq = __builtin_amdgcn_permlane16_swap(asu(rstd), asu(rstd), false, false);
    const v2u ones2 = {0x3F803F80u, 0x3F803F80u};
    auto attend = [&](int t, int hd, v2u kA, v2u vA, bool prev_ok) {
      const float sc = SC * asf(rq[t]);
      f32x4 s0 = mfma16k16(kA, qp_[t][hd], f32x4{});
      f32x4 s1 = mfma16k16(kp[t][hd], qp_[t][hd], f32x4{});
      float e[8];
#pragma unroll
      for (int r = 0; r < 4; ++r) {
        e[r] = __builtin_fmaf(s0[r], sc, prev_ok ? bias0[r] : NEG);
        e[4 + r] = __builtin_fmaf(s1[r], sc, bias1[r]);
      }
      float mx = fmaxf(fmaxf(fmaxf(e[0], e[1]), fmaxf(e[2], e[3])), fmaxf(fmaxf(e[4], e[5]), fmaxf(e[6], e[7])));
      mx = xmax32(xmax16(mx));
#pragma unroll
      for (int r = 0; r < 8; ++r) e[r] = __builtin_amdgcn_exp2f(e[r] - mx);
      const v2u p0 = {pack2(e[0], e[1]), pack2(e[2], e[3])}, p1 = {pack2(e[4], e[5]), pack2(e[6], e[7])};
      f32x4 sm = mfma16k16(ones2, p0, f32x4{});
      sm = mfma16k16(ones2, p1, sm);
      f32x4 o = mfma16k16(vA, p0, f32x4{});
      o = mfma16k16(vp[t][hd], p1, o);
      const float inv = __builtin_amdgcn_rcpf(sm[0]);
      return v2u{pack2(o[0] * inv, o[1] * inv), pack2(o[2] * inv, o[3] * inv)};
    };
    {
      const bool ok = T0 + 32 * w > 0;
      const v2u o10 = attend(1, 0, kp[0][0], vp[0][0], true), o11 = attend(1, 1, kp[0][1], vp[0][1], true);
      const v2u o00 = attend(0, 0, kprev[0], vprev[0], ok), o01 = attend(0, 1, kprev[1], vprev[1], ok);
      stsc1(rsW, O_OFF + (u32)(((g * 8 + 2 * w + 1) * 16 + m) * 1024 + lane * 16), v4u{o10.x, o10.y, o11.x, o11.y});
      stsc1(rsW, O_OFF + (u32)(((g * 8 + 2 * w) * 16 + m) * 1024 + lane * 16), v4u{o00.x, o00.y, o01.x, o01.y});
    }
  }
  STAMP(2);
  WAITVN(0);
  publish(fl, m, tag);
  STAMP(3);

  {
    bf16x8 w2[8][4];
    int a[4];
    {
      const int row = 4 * q + qp, f = ((row >> 1) & 3) << 1;
#pragma unroll
      for (int n = 0; n < 4; ++n) a[n] = opq(W2_OFF + row * 128 + (((2 * n + (pp >> 1)) ^ f) * 16) + (pp & 1) * 8);
#pragma unroll
      for (int s = 0; s < 8; ++s)
#pragma unroll
        for (int n = 0; n < 4; ++n) w2[s][n] = tr2(a[n] + s * 4096, 2048);
    }
    poll(fl, m, 16, tag);
    BAR();
    STAMP(4);
    v4u of[16];
    const u32 ob = O_OFF + (u32)((g * 8 + 4 * th + w) * 16) * 1024;
    sfor<0, 16>([&](auto i) { ldsc1<0>(of[i.v], rsW, lane * 16, ob + (u32)((i.v + 2 * ct) & 15) * 1024); });
    f32x4 acc[4] = {};
    sfor<0, 16>([&](auto sc) {
      constexpr int s = sc.v;
      wait_for<15 - s>(of[s]);
      const bf16x8 bfr = __builtin_bit_cast(bf16x8, of[s]);
#pragma unroll
      for (int n = 0; n < 4; ++n) {
        acc[n] = mfma16(w2[s & 7][n], bfr, acc[n]);
        if constexpr (s < 8) w2[s][n] = tr2(a[n] + (s + 8) * 4096, 2048);
      }
      SCHED();
    });
    STAMP(5);
    float s = 0.f, ss = 0.f;
    const int tl = 16 * w + tok;
    const int t32 = g * 4 + 2 * th + (w >> 1);
    const u32 hoff = H_OFF + (u32)((t32 * 32 + 4 * ct) * 1024 + (16 * (w & 1) + tok + 32 * (q >> 1)) * 16 + (q & 1) * 8);
#pragma unroll
    for (int n = 0; n < 4; ++n) {
      const u32 h01 = pack2(bflo(xres[n].x) + acc[n][0], bfhi(xres[n].x) + acc[n][1]);
      const u32 h23 = pack2(bflo(xres[n].y) + acc[n][2], bfhi(xres[n].y) + acc[n][3]);
      const v2u hp = {h01, h23};
      stsc1_8(rsW, hoff + n * 1024, hp);
      *lp<v2u>(M_HK + tl * HK_PITCH + (16 * n + 4 * q) * 2) = hp;
      const float a0 = bflo(h01), a1 = bfhi(h01), a2 = bflo(h23), a3 = bfhi(h23);
      s += (a0 + a1) + (a2 + a3);
      ss = __builtin_fmaf(a0, a0, ss); ss = __builtin_fmaf(a1, a1, ss); ss = __builtin_fmaf(a2, a2, ss); ss = __builtin_fmaf(a3, a3, ss);
    }
    s = xadd32(xadd16(s)); ss = xadd32(xadd16(ss));
    if (q == 0) stsc1_8(rsW, ST_OFF + (u32)((ct * S + T0 + 64 * th + tl) * 8), v2u{asu(s), asu(ss)});
  }
  WAITVN(0);
  WAITL0();
  publish(fl + 256, m, tag);
  STAMP(6);

  {
    const int wa = (8 * hh + qp) * 192 + (q & 1) * 32 + pp * 8;
    bf16x8 wA[6], wB[6];
    auto w3_read1 = [&](bf16x8& dst, int base, int i) { const int o = base + (i & 1) * 12288 + (i >> 1) * 64; dst = tr2(o, 768); };
    poll(fl + 256, m, 16, tag);
    BAR();
    STAMP(7);
    const int t32 = g * 4 + w;
    v2u st[4];
#pragma unroll
    for (int i = 0; i < 4; ++i) ldsc1_8(st[i], rsW, (u32)(((hh * 4 + i) * S + l32) * 8), ST_OFF + (u32)(t32 * 32) * 8);
    v4u hf[16];
    const u32 hb = H_OFF + (u32)(t32 * 32) * 1024;
    auto ldh = [&](auto i) { ldsc1<(i.v & 3) * 1024>(hf[i.v & 15], rsW, lane * 16, hb + (u32)(((i.v >> 2) + m) & 7) * 4096); };
    sfor<0, 16>(ldh);
#pragma unroll
    for (int i = 0; i < 6; ++i) w3_read1(wA[i], wa, i);
    asm volatile("s_waitcnt vmcnt(16)" : "+v"(st[0]), "+v"(st[1]), "+v"(st[2]), "+v"(st[3])::"memory");
    float rs, nb;
    {
      float s = (asf(st[0].x) + asf(st[1].x)) + (asf(st[2].x) + asf(st[3].x)), ss = (asf(st[0].y) + asf(st[1].y)) + (asf(st[2].y) + asf(st[3].y));
      s = xadd32(s); ss = xadd32(ss);
      const float mu = s * (1.0f / D);
      rs = __builtin_amdgcn_rsqf(fmaxf(__builtin_fmaf(-mu, mu, ss * (1.0f / D)), 0.f) + 1e-5f);
      nb = -mu * rs;
    }
    auto ln = [&](v4u h, int d) {
      const f32x2 v = {bflo(h[d]), bfhi(h[d])};
      const f32x2 r = __builtin_elementwise_fma(v, f32x2{rs, rs}, f32x2{nb, nb});
      return pack2(r[0], r[1]);
    };
    v4u yA, yB;
    wait_for<15>(hf[0]);
#pragma unroll
    for (int d = 0; d < 4; ++d) yA[d] = ln(hf[0], d);
    ldh(IC<16>{});
    f32x16 acc[6] = {};
    auto step = [&](auto sc, bf16x8(&wc)[6], bf16x8(&wn)[6], v4u& yc, v4u& yn) {
      constexpr int s = sc.v, c1 = (s + 1) >> 2;
      if constexpr (s + 1 < 32) wait_for<(16 + s < 31 ? 16 + s : 31) - (s + 1)>(hf[(s + 1) & 15]);
      if constexpr (s >= 4 && (s & 3) == 0) BAR();
      const int base = opq(wa + (c1 % 6) * SLOT + (16 * ((s + 1) & 3)) * 192);
      const bf16x8 yb = __builtin_bit_cast(bf16x8, yc);
      __builtin_amdgcn_s_waitcnt(0xC07F);
      sfor<0, 6>([&](auto ic) {
        constexpr int i = ic.v;
        acc[i] = mfma32(wc[i], yb, acc[i]);
        if constexpr (s + 1 < 32) {
          if constexpr (i < 3) { w3_read1(wn[2 * i], base, 2 * i); w3_read1(wn[2 * i + 1], base, 2 * i + 1); }
          if constexpr (i >= 2) yn[i - 2] = ln(hf[(s + 1) & 15], i - 2);
        }
        if constexpr (i == 5 && 17 + s < 32) ldh(IC<(17 + s) & 31>{});
        SCHED();
      });
    };
    sfor<0, 14>([&](auto k) { step(IC<2 * k.v>{}, wA, wB, yA, yB); step(IC<2 * k.v + 1>{}, wB, wA, yB, yA); });
    {
      BAR();
      v4u y4[4];
      y4[0] = yA;
      sfor<1, 4>([&](auto j) {
        wait_for<3 - j.v>(hf[(28 + j.v) & 15]);
#pragma unroll
        for (int d = 0; d < 4; ++d) y4[j.v][d] = ln(hf[(28 + j.v) & 15], d);
      });
      const int base = opq(wa + (7 % 6) * SLOT);
      auto epi = [&](int i, int s) {
        v4u o;
#pragma unroll
        for (int d = 0; d < 4; ++d) {
          const int r = 8 * s + 2 * d;
          o[d] = pack2(fmaxf(acc[2 * i][r], 0.f) * acc[2 * i + 1][r], fmaxf(acc[2 * i][r + 1], 0.f) * acc[2 * i + 1][r + 1]);
        }
        const int f0 = 96 * m + 32 * i;
        if (f0 < F) stsc1(rsW, A_OFF + (u32)((t32 * 86 + f0 / 16 + s) * 1024 + lane * 16), o);
      };
      bf16x8 wl[3][8];
      auto rd_pair = [&](int i) {
#pragma unroll
        for (int j = 0; j < 4; ++j) { w3_read1(wl[i][2 * j], base + 16 * j * 192, 2 * i); w3_read1(wl[i][2 * j + 1], base + 16 * j * 192, 2 * i + 1); }
      };
      rd_pair(0); rd_pair(1);
      SCHED();
      sfor<0, 3>([&](auto ic) {
        constexpr int i = ic.v;
        if constexpr (i == 0) rd_pair(2);
        sfor<0, 4>([&](auto jc) {
          constexpr int j = jc.v;
          const bf16x8 yb = __builtin_bit_cast(bf16x8, y4[j]);
          acc[2 * i] = mfma32(wl[i][2 * j], yb, acc[2 * i]);
          acc[2 * i + 1] = mfma32(wl[i][2 * j + 1], yb, acc[2 * i + 1]);
          if constexpr (i > 0 && j == 1) epi(i - 1, 0);
          if constexpr (i > 0 && j == 3) epi(i - 1, 1);
          SCHED();
        });
      });
      STAMP(8);
      BAR();
      epi(2, 0); epi(2, 1);
    }
    WAITVN(0);
  }
  publish(fl + 512, m, tag);
  STAMP(9);

  {
    poll(fl + 512, m, 15, tag);
    BAR();
    STAMP(10);
    const int mh = w & 1, kh = w >> 1;
    const int t32 = g * 4 + 2 * th + mh;
    v4u af[36];
    const u32 ab = A_OFF + (u32)(t32 * 86 + kh * 43) * 1024;
    auto lda = [&](auto i) { const int ps = i.v + 5 * ct; ldsc1<0>(af[i.v % 36], rsW, lane * 16, ab + (u32)(ps >= 43 ? ps - 43 : ps) * 1024); };
    sfor<0, 14>(lda);
    f32x16 acc[2] = {};
    int wa[2];
#pragma unroll
    for (int n = 0; n < 2; ++n) wa[n] = (4 * hh + qp) * 128 + ((n ^ ((qp >> 1) & 1)) * 64) + (q & 1) * 32 + pp * 8;
    bf16x8 wA[2], wB[2];
    auto w4_read = [&](bf16x8(&dst)[2], auto sc) {
      constexpr int s = sc.v;
      const int off = s < 42 ? ((s / 6 + 2) % 6) * SLOT + kh * 12288 + (s % 6) * 2048 : M_W4T + kh * 2048;
#pragma unroll
      for (int n = 0; n < 2; ++n) dst[n] = tr2(opq(wa[n] + off), 1024);
    };
    w4_read(wA, IC<0>{});
    auto step = [&](auto sc, bf16x8(&wc)[2], bf16x8(&wn)[2]) {
      constexpr int s = sc.v;
      constexpr int issued = (14 + 2 * (s < 8 ? s : 8) + (s > 8 ? s - 8 : 0)) < 43 ? (14 + 2 * (s < 8 ? s : 8) + (s > 8 ? s - 8 : 0)) : 43;
      wait_for<issued - 1 - s>(af[s % 36]);
      if constexpr (s == 6 || s == 30) BAR();
      if constexpr (s + 1 < 43) w4_read(wn, IC<s + 1>{});
      const bf16x8 bfr = __builtin_bit_cast(bf16x8, af[s % 36]);
      acc[0] = mfma32(wc[0], bfr, acc[0]);
      if constexpr (issued < 43) lda(IC<(issued < 43 ? issued : 0)>{});
      SCHED();
      acc[1] = mfma32(wc[1], bfr, acc[1]);
      if constexpr (s < 8 && issued + 1 < 43) lda(IC<(issued + 1 < 43 ? issued + 1 : 0)>{});
      SCHED();
    };
    sfor<0, 21>([&](auto k) { step(IC<2 * k.v>{}, wA, wB); step(IC<2 * k.v + 1>{}, wB, wA); });
    step(IC<42>{}, wA, wB);
    STAMP(11);
    LAS f32x4* red = lp<f32x4>(3 * SLOT);
    {
      const f32x16 snd = kh ? acc[0] : acc[1];
#pragma unroll
      for (int gg = 0; gg < 4; ++gg) red[(w * 4 + gg) * 64 + lane] = f32x4{snd[4 * gg], snd[4 * gg + 1], snd[4 * gg + 2], snd[4 * gg + 3]};
    }
    if (m == 0 && tid == 0) __hip_atomic_store(ep, tag, RLX_AGENT);
    WAITL0(); BAR();
    {
      const f32x16 keep = kh ? acc[1] : acc[0];
      const int pw = w ^ 2;
      const rsrc_t rsO = mkrs(p.out, S * D * 2);
      v2u r[4];
#pragma unroll
      for (int gg = 0; gg < 4; ++gg) {
        const f32x4 o = red[(pw * 4 + gg) * 64 + lane];
        const v2u hk = *lp<v2u>(M_HK + (32 * mh + l32) * HK_PITCH + (32 * kh + 8 * gg + 4 * hh) * 2);
        r[gg].x = pack2(bflo(hk.x) + (keep[4 * gg] + o[0]), bfhi(hk.x) + (keep[4 * gg + 1] + o[1]));
        r[gg].y = pack2(bflo(hk.y) + (keep[4 * gg + 2] + o[2]), bfhi(hk.y) + (keep[4 * gg + 3] + o[3]));
      }
      const u32 ob = (u32)(T0 + 64 * th + 32 * mh + l32) * (D * 2) + (u32)(64 * ct + 32 * kh + 8 * hh) * 2;
#pragma unroll
      for (int gp = 0; gp < 2; ++gp) {
        const auto sx = __builtin_amdgcn_permlane32_swap(r[2 * gp].x, r[2 * gp + 1].x, false, false);
        const auto sy = __builtin_amdgcn_permlane32_swap(r[2 * gp].y, r[2 * gp + 1].y, false, false);
        __builtin_amdgcn_raw_buffer_store_b128(v4u{sx[0], sy[0], sx[1], sy[1]}, rsO, ob + gp * 32, 0, 0);
      }
    }
  }
  STAMP(12);
}

extern "C" void kernel_launch(void* const* d_in, const int* in_sizes, int n_in, void* d_out, int out_size,
                              void* d_ws, size_t ws_size, hipStream_t stream) {
  Params p;
  p.x = (const u16*)d_in[0]; p.wqkv = (const u16*)d_in[1]; p.wo = (const u16*)d_in[2];
  p.wg = (const u16*)d_in[4]; p.wu = (const u16*)d_in[5]; p.wd = (const u16*)d_in[6];
  p.out = (u16*)d_out; p.ws = (unsigned char*)d_ws;
  (void)hipFuncSetAttribute((const void*)fused_block, hipFuncAttributeMaxDynamicSharedMemorySize, LDS_BYTES);
  fused_block<<<256, 512, LDS_BYTES, stream>>>(p);
}
```

```cpp
#include <hip/hip_runtime.h>
#include <stdint.h>

typedef unsigned short u16;
typedef unsigned u32;
typedef unsigned long long u64;
typedef short bf16x8 __attribute__((ext_vector_type(8)));
typedef short bf16x4 __attribute__((ext_vector_type(4)));
typedef float f32x2 __attribute__((ext_vector_type(2)));
typedef float f32x4 __attribute__((ext_vector_type(4)));
typedef float f32x16 __attribute__((ext_vector_type(16)));
typedef unsigned v4u __attribute__((ext_vector_type(4)));
typedef unsigned v2u __attribute__((ext_vector_type(2)));
typedef __bf16 bf2_t __attribute__((ext_vector_type(2)));
#define GAS __attribute__((address_space(1)))
#define LAS __attribute__((address_space(3)))
#define DI __device__ __forceinline__
#define RLX_AGENT __ATOMIC_RELAXED, __HIP_MEMORY_SCOPE_AGENT
typedef GAS u64 gu64;
typedef __amdgpu_buffer_rsrc_t rsrc_t;

constexpr int S = 2048, D = 512, F = 1376;
constexpr u32 O_OFF = 0;
constexpr u32 H_OFF = 2u << 20;
constexpr u32 A_OFF = 4u << 20;
constexpr u32 ST_OFF = 10u << 20;
constexpr u32 ST1_OFF = (10u << 20) + (512u << 10);
constexpr u32 FL_OFF = 11u << 20;
constexpr u32 EP_OFF = 12u << 20;
constexpr u32 WS_BYTES = 13u << 20;

constexpr int SLOT = 24576;
constexpr int MISC = 6 * SLOT;
constexpr int LDS_BYTES = 163840;
constexpr int P1_STAGE = 30720;
constexpr int P1_WOFF = 18432;
constexpr int W2_OFF = 3 * P1_STAGE;
constexpr int M_CS = 157696;
constexpr int M_MR = M_CS + 1024;
constexpr int KVH_OFF = M_CS + 2048;
constexpr int KV_OFF = 3 * SLOT;
constexpr int M_HK = MISC;
constexpr int M_W4T = MISC + 8704;
constexpr int HK_PITCH = 136;

struct Params {
  const u16* x; const u16* wqkv; const u16* wo; const u16* wg; const u16* wu; const u16* wd;
  u16* out; unsigned char* ws;
};

template <int I> struct IC { static constexpr int v = I; };
template <int I, int N, class Fn> DI void sfor(Fn&& f) { if constexpr (I < N) { f(IC<I>{}); sfor<I + 1, N>(f); } }

DI u32 pack2(float a, float b) { f32x2 v = {a, b}; bf2_t r = __builtin_convertvector(v, bf2_t); return __builtin_bit_cast(u32, r); }
DI float bflo(u32 u) { return __builtin_bit_cast(float, u << 16); }
DI float bfhi(u32 u) { return __builtin_bit_cast(float, u & 0xffff0000u); }
DI float asf(u32 u) { return __builtin_bit_cast(float, u); }
DI u32 asu(float f) { return __builtin_bit_cast(u32, f); }
DI f32x4 mfma16(bf16x8 a, bf16x8 b, f32x4 c) { return __builtin_amdgcn_mfma_f32_16x16x32_bf16(a, b, c, 0, 0, 0); }
DI f32x4 mfma16k16(v2u a, v2u b, f32x4 c) { return __builtin_amdgcn_mfma_f32_16x16x16bf16_1k(__builtin_bit_cast(bf16x4, a), __builtin_bit_cast(bf16x4, b), c, 0, 0, 0); }
DI f32x16 mfma32(bf16x8 a, bf16x8 b, f32x16 c) { return __builtin_amdgcn_mfma_f32_32x32x16_bf16(a, b, c, 0, 0, 0); }
DI bf16x8 cat(bf16x4 a, bf16x4 b) { return __builtin_shufflevector(a, b, 0, 1, 2, 3, 4, 5, 6, 7); }
#pragma clang diagnostic ignored "-Wint-to-pointer-cast"
DI bf16x4 trr(int off) { return __builtin_amdgcn_ds_read_tr16_b64_v4i16((LAS bf16x4*)(u32)off); }
DI bf16x8 tr2(int off, int d) { return cat(trr(off), trr(off + d)); }
DI bf16x8 ldsb(int off) { return *(LAS bf16x8*)(u32)off; }
template <class T> DI LAS T* lp(int off) { return (LAS T*)(u32)off; }
DI rsrc_t mkrs(const void* p, u32 bytes) { return __builtin_amdgcn_make_buffer_rsrc((void*)p, 0, bytes, 0x00020000); }
DI u32 rfl(u32 v) { return __builtin_amdgcn_readfirstlane(v); }
DI int opq(int v) { asm volatile("" : "+v"(v)); return v; }
DI float xmax16(float v) { auto r = __builtin_amdgcn_permlane16_swap(asu(v), asu(v), false, false); return fmaxf(asf(r[0]), asf(r[1])); }
DI float xmax32(float v) { auto r = __builtin_amdgcn_permlane32_swap(asu(v), asu(v), false, false); return fmaxf(asf(r[0]), asf(r[1])); }
DI float xadd16(float v) { auto r = __builtin_amdgcn_permlane16_swap(asu(v), asu(v), false, false); return asf(r[0]) + asf(r[1]); }
DI float xadd32(float v) { auto r = __builtin_amdgcn_permlane32_swap(asu(v), asu(v), false, false); return asf(r[0]) + asf(r[1]); }
#define BAR() __builtin_amdgcn_s_barrier()
#define SCHED() __builtin_amdgcn_sched_barrier(0)
#define WAITVN(n) asm volatile("s_waitcnt vmcnt(%0)" ::"i"(n) : "memory")
#define WAITL0() asm volatile("s_waitcnt lgkmcnt(0)" ::: "memory")

DI void dma(rsrc_t rs, u32 voff, u32 soff, u32 ldsaddr) {
  asm volatile("s_mov_b32 m0, %0\n\ts_nop 0\n\tbuffer_load_dwordx4 %1, %2, %3 offen lds" ::"s"(ldsaddr), "v"(voff), "s"(rs), "s"(soff) : "memory");
}
template <int IMM> DI void ldsc1(v4u& dst, rsrc_t rs, u32 voff, u32 soff) {
  asm volatile("buffer_load_dwordx4 %0, %1, %2, %3 offen offset:%4 sc1" : "=v"(dst) : "v"(voff), "s"(rs), "s"(soff), "i"(IMM) : "memory");
}
DI void ldsc1_8(v2u& dst, rsrc_t rs, u32 voff, u32 soff) {
  asm volatile("buffer_load_dwordx2 %0, %1, %2, %3 offen sc1" : "=v"(dst) : "v"(voff), "s"(rs), "s"(soff) : "memory");
}
DI void ld16(v4u& dst, rsrc_t rs, u32 voff, u32 soff) {
  asm volatile("buffer_load_dwordx4 %0, %1, %2, %3 offen" : "=v"(dst) : "v"(voff), "s"(rs), "s"(soff) : "memory");
}
DI void ld4_keep(u32& dst, rsrc_t rs, u32 voff) { asm volatile("buffer_load_dword %0, %1, %2, 0 offen" : "+v"(dst) : "v"(voff), "s"(rs) : "memory"); }
template <int IMM> DI void ld8(v2u& dst, rsrc_t rs, u32 voff, u32 soff) {
  asm volatile("buffer_load_dwordx2 %0, %1, %2, %3 offen offset:%4" : "=v"(dst) : "v"(voff), "s"(rs), "s"(soff), "i"(IMM) : "memory");
}
template <int N> DI void wait_for(v4u& a) { asm volatile("s_waitcnt vmcnt(%1)" : "+v"(a) : "i"(N > 63 ? 63 : N) : "memory"); }
DI void stsc1(rsrc_t rs, u32 off, v4u v) { __builtin_amdgcn_raw_buffer_store_b128(v, rs, off, 0, 16); }
DI void stsc1_8(rsrc_t rs, u32 off, v2u v) { __builtin_amdgcn_raw_buffer_store_b64(v, rs, off, 0, 16); }


DI void publish(gu64* fl, int m, u64 tag) {
  BAR();
  if (threadIdx.x < 16) __hip_atomic_store(fl + threadIdx.x * 16 + m, tag, RLX_AGENT);
}
DI void poll(gu64* fl, int m, int nprod, u64 tag) {
  if (threadIdx.x < 64) {
    const int lane = threadIdx.x;
    for (u32 spins = 0; spins < (1u << 20); ++spins) {
      u64 v = tag;
      if (lane < nprod) v = __hip_atomic_load(fl + m * 16 + lane, RLX_AGENT);
      if (__all(v == tag)) break;
    }
    __builtin_amdgcn_fence(__ATOMIC_ACQUIRE, "wavefront");
  }
}

__global__ __launch_bounds__(512, 2) void fused_block(Params p) {
  extern __shared__ __attribute__((aligned(16))) unsigned char smem[];
  const int tid = threadIdx.x, lane = tid & 63, w8 = (int)rfl(tid >> 6), w = w8 & 3;
  const int b = blockIdx.x, g = ((b & 7) >> 1) * 4 + ((b >> 3) >> 3), m = (b & 1) * 8 + ((b >> 3) & 7);
  const int T0 = g * 128;
  const int tok = lane & 15, q = lane >> 4;
  const int th = m >> 3, ct = m & 7;
  const rsrc_t rsW = mkrs(p.ws, WS_BYTES);
  const rsrc_t rsX = mkrs(p.x, S * D * 2);
  const u32 halo0 = (u32)(T0 >= 16 ? T0 - 16 : 0);
  asm volatile("" ::"s"((u32)(size_t)smem));

  if (w8 >= 4) {
    const rsrc_t rsW1 = mkrs(p.wqkv, D * 3 * D * 2);
    v2u epv;
    ldsc1_8(epv, rsW, 0, EP_OFF + g * 128);
    u32 vx0, vx1, vxh, vw0, vw1, vw2;
    {
      const int r8 = lane >> 3, sl = lane & 7;
      vx0 = r8 * 1024 + ((sl ^ ((lane >> 4) & 7)) * 16);
      vx1 = r8 * 1024 + ((sl ^ ((4 + (lane >> 4)) & 7)) * 16);
      vxh = (w & 1) ? vx1 : vx0;
      auto pat = [&](int j) { const int e = j * 64 + lane, r = e / 12, pc = e % 12; return (u32)(r * 3072 + (pc >> 2) * 1024 + (pc & 3) * 16); };
      vw0 = pat(0); vw1 = pat(1); vw2 = pat(2);
    }
    const u32 sxh = (halo0 + 8 * (w & 1)) * 1024, sxm = (u32)(T0 + 32 * w) * 1024, sw1 = (u32)(16 * w) * 3072 + m * 64;
    const int rot = (g + m) & 7;
    auto p1_piece = [&](int cc, int j) {
      const u32 lb = (cc % 5) * P1_STAGE;
      const u32 c = (u32)((cc + rot) & 7);
      if (j == 0) dma(rsX, vxh, sxh + c * 128, lb + (w & 1) * 1024);
      if (j == 1) dma(rsW1, vw0, sw1 + c * (64 * 3072), lb + P1_WOFF + (3 * w) * 1024);
      if (j == 2) dma(rsX, vx0, sxm + c * 128, lb + (2 + 4 * w) * 1024);
      if (j == 3) dma(rsW1, vw1, sw1 + c * (64 * 3072), lb + P1_WOFF + (3 * w + 1) * 1024);
      if (j == 4) dma(rsX, vx1, sxm + c * 128 + 8192, lb + (3 + 4 * w) * 1024);
      if (j == 5) dma(rsW1, vw2, sw1 + c * (64 * 3072), lb + P1_WOFF + (3 * w + 2) * 1024);
      if (j == 6) dma(rsX, vx0, sxm + c * 128 + 16384, lb + (4 + 4 * w) * 1024);
      if (j == 7) dma(rsX, vx1, sxm + c * 128 + 24576, lb + (5 + 4 * w) * 1024);
    };
    const rsrc_t rsW2 = mkrs(p.wo, D * D * 2), rsG = mkrs(p.wg, D * F * 2), rsU = mkrs(p.wu, D * F * 2), rsD = mkrs(p.wd, F * D * 2);
    const u32 v2 = (lane >> 3) * 1024 + (((lane & 7) ^ (((lane >> 4) & 3) << 1)) * 16);
    auto w2_piece = [&](int i) {
      const int ii = 16 * w + i, ls = ii >> 2;
      dma(rsW2, v2, (u32)(32 * ((ls + 2 * ct) & 15) + 8 * (ii & 3)) * 1024 + ct * 128, W2_OFF + ii * 1024);
    };
    const int qp = (lane & 15) >> 2, pp = lane & 3;
    f32x4 ahA = {}, ahB = {};
    const int xha0 = tok * 128 + ((q ^ (tok >> 1)) * 16), xha1 = tok * 128 + (((4 + q) ^ (tok >> 1)) * 16);
    const int wha = P1_WOFF + (8 * q + qp) * 192 + (1 + (w >> 1)) * 64 + (w & 1) * 32 + pp * 8;
    const int whb = P1_WOFF + (8 * q + qp) * 192 + (w < 2 ? 0 : 128) + (w & 1) * 32 + pp * 8;
    auto halo_with = [&](int hc, auto&& issue) {
      const int sb = (hc % 5) * P1_STAGE;
      v4u xh[2]; bf16x8 fa[2], fb[2];
#pragma unroll
      for (int s2 = 0; s2 < 2; ++s2) {
        xh[s2] = __builtin_bit_cast(v4u, ldsb((s2 ? xha1 : xha0) + sb));
        fa[s2] = tr2(wha + sb + s2 * 6144, 768); fb[s2] = tr2(whb + sb + s2 * 6144, 768);
      }
      SCHED();
      issue(0); issue(1); issue(2); issue(3);
      SCHED();
#pragma unroll
      for (int s2 = 0; s2 < 2; ++s2) {
#pragma unroll
        for (int d = 0; d < 4; ++d) xh[s2][d] = tok == 0 ? 0x3F803F80u : xh[s2][d];
        const bf16x8 xhb = __builtin_bit_cast(bf16x8, xh[s2]);
        ahA = mfma16(fa[s2], xhb, ahA);
        ahB = mfma16(fb[s2], xhb, ahB);
      }
      SCHED();
      issue(4); issue(5); issue(6); issue(7);
    };
    sfor<0, 24>([&](auto i) { p1_piece(i.v >> 3, i.v & 7); });
    WAITVN(16); BAR();
    halo_with(0, [&](int j) { p1_piece(3, j); });
    WAITVN(16); BAR();
    halo_with(1, [&](int j) { p1_piece(4, j); });
    WAITVN(16); BAR();
    halo_with(2, [&](int j) { p1_piece(5, j); });
    WAITVN(16); BAR();
    halo_with(3, [&](int j) { p1_piece(6, j); });
    WAITVN(16); BAR();
    halo_with(4, [&](int j) { p1_piece(7, j); });
    u32 touch = 0;
    {
      const u32 wg_ = (u32)(b * 4 + w);
      const u32 o4 = (u32)lane < 4u ? (wg_ * 4u + lane) * 128u : 0xFFFFFF00u, o11 = (u32)lane < 11u ? (wg_ * 11u + lane) * 128u : 0xFFFFFF00u;
      ld4_keep(touch, rsW2, o4); ld4_keep(touch, rsG, o11); ld4_keep(touch, rsU, o11); ld4_keep(touch, rsD, o11);
    }
    v4u sth;
    ldsc1<0>(sth, rsW, ST1_OFF + (u32)(g * 4096 + tok * 16), 0);
    WAITVN(21); BAR();
    halo_with(5, [&](int) {});
    WAITVN(13); BAR();
    halo_with(6, [&](int) {});
    WAITVN(5); BAR();
    halo_with(7, [&](int j) { w2_piece(j); });
    asm volatile("s_waitcnt vmcnt(8)" : "+v"(sth), "+v"(touch), "+v"(epv)::"memory");
    const u64 tag = (((u64)rfl(epv.y) << 32) | rfl(epv.x)) + 1;
    const u32 tag32 = (u32)tag ^ (u32)(tag >> 32) ^ 0x9E3779B9u;
    for (u32 spins = 0; spins < (1u << 20); ++spins) {
      if (__all(sth.y == tag32 && sth.w == tag32)) break;
      __builtin_amdgcn_s_sleep(1);
      ldsc1<0>(sth, rsW, ST1_OFF + (u32)(g * 4096 + tok * 16), 0);
      wait_for<0>(sth);
    }
    {
      LAS float* cs_ = lp<float>(M_CS);
      if (tok == 0) {
        *(LAS f32x4*)(cs_ + 32 + 16 * w + 4 * q) = ahA;
        if (w < 2) *(LAS f32x4*)(cs_ + 16 * w + 4 * q) = ahB;
      }
      const float hmu = asf(sth.x), hrs = asf(sth.z);
      float hv[4];
#pragma unroll
      for (int r = 0; r < 4; ++r) {
        const float c = asf(__builtin_amdgcn_update_dpp(0, asu(ahA[r]), 0x150, 0xf, 0xf, false));
        hv[r] = hrs * __builtin_fmaf(-hmu, c, ahA[r]);
      }
      const u32 o0 = pack2(hv[0], hv[1]), o1 = pack2(hv[2], hv[3]);
      if (w < 2) *lp<v2u>(KVH_OFF + w * 512 + (tok + 16 * (((q & 1) << 1) | (q >> 1))) * 8) = v2u{o0, o1};
      else {
        LAS u16* vv = lp<u16>(KVH_OFF + w * 512) + (4 * q + 16 * (tok >> 2)) * 4 + (tok & 3);
        vv[0] = (u16)o0; vv[4] = (u16)(o0 >> 16); vv[8] = (u16)o1; vv[12] = (u16)(o1 >> 16);
      }
    }
    WAITL0();
    const u32 v30 = (lane / 12) * (F * 2) + (lane % 12) * 16, v31 = ((64 + lane) / 12) * (F * 2) + ((64 + lane) % 12) * 16,
              v32 = ((128 + lane) / 12) * (F * 2) + ((128 + lane) % 12) * 16;
    const u32 s3 = (u32)(16 * w) * (F * 2) + m * 192;
    auto w3_chunk = [&](int c) {
      const u32 so = s3 + (u32)(64 * ((c + m) & 7)) * (F * 2), lb = (c % 6) * SLOT + 3 * w * 1024;
      dma(rsG, v30, so, lb); dma(rsG, v31, so, lb + 1024); dma(rsG, v32, so, lb + 2048);
      dma(rsU, v30, so, lb + 12288); dma(rsU, v31, so, lb + 12288 + 1024); dma(rsU, v32, so, lb + 12288 + 2048);
    };
    const u32 v4s = (lane >> 3) * 1024 + (((lane & 7) ^ (((lane >> 4) & 1) << 2)) * 16);
    auto w4_row = [&](int ls) { const int ps = ls + 5 * ct; return (u32)((ps >= 43 ? ps - 43 : ps) * 16); };
    auto w4_part = [&](int part) {
      const u32 lb = ((part + 2) % 6) * SLOT + 3 * w * 1024;
#pragma unroll
      for (int j = 0; j < 3; ++j) {
        const int jj = 3 * w + j;
        const u32 so = (w4_row(6 * part + (jj >> 1)) + (jj & 1) * 8) * 1024 + ct * 128;
        dma(rsD, v4s, so, lb + j * 1024); dma(rsD, v4s, so + 688 * 1024, lb + 12288 + j * 1024);
      }
    };
    BAR();
    sfor<8, 16>([&](auto i) { w2_piece(i.v); });
    BAR();
    w3_chunk(0); w3_chunk(1);
    WAITVN(12);
    BAR();
    w3_chunk(2);
    BAR();
    BAR();
    w3_chunk(3);
    WAITVN(6); BAR();
    w3_chunk(4); w3_chunk(5);
    BAR(); w3_chunk(6);
    WAITVN(18); BAR(); w3_chunk(7);
    WAITVN(18); BAR(); w4_part(0);
    WAITVN(18); BAR(); w4_part(1);
    WAITVN(18); BAR(); w4_part(2);
    WAITVN(18); BAR(); w4_part(3);
    BAR(); w4_part(4);
    BAR();
    w4_part(5);
    dma(rsD, v4s, ((u32)(w >> 1) * 688 + w4_row(42) + (w & 1) * 8) * 1024 + ct * 128, M_W4T + w * 1024);
    BAR();
    WAITVN(7); BAR();
    WAITVN(0); BAR(); w4_part(6);
    WAITVN(0); BAR();
    BAR();
    return;
  }

  __builtin_amdgcn_s_setprio(2);
  gu64* ep = (gu64*)(p.ws + EP_OFF) + g * 16;
  gu64* fl = (gu64*)(p.ws + FL_OFF) + g * 768;
  v2u epv, xres[4];
  v4u srow[3];
  ldsc1_8(epv, rsW, 0, EP_OFF + g * 128);
  sfor<0, 3>([&](auto jc) {
    const int j = jc.v < 2 ? w + 4 * jc.v : 8, R = 9 * m + j;
    const u32 token = R < 16 ? halo0 + R : (u32)(T0 + R - 16);
    ld16(srow[jc.v], rsX, lane * 16, token * 1024);
  });
  {
    const u32 vo = (u32)tok * 1024 + q * 8, so = (u32)(T0 + 64 * th + 16 * w) * 1024 + ct * 128;
    ld8<0>(xres[0], rsX, vo, so); ld8<32>(xres[1], rsX, vo, so); ld8<64>(xres[2], rsX, vo, so); ld8<96>(xres[3], rsX, vo, so);
  }
  asm volatile("s_waitcnt vmcnt(4)" : "+v"(epv), "+v"(srow[0]), "+v"(srow[1]), "+v"(srow[2])::"memory");
  const u64 tag = (((u64)rfl(epv.y) << 32) | rfl(epv.x)) + 1;
  const u32 tag32 = (u32)tag ^ (u32)(tag >> 32) ^ 0x9E3779B9u;
    {
    float sv[3], qv[3];
#pragma unroll
    for (int j = 0; j < 3; ++j) {
      float s = 0.f, ss = 0.f;
#pragma unroll
      for (int d = 0; d < 4; ++d) {
        const float a = bflo(srow[j][d]), c = bfhi(srow[j][d]);
        s += a + c; ss = __builtin_fmaf(a, a, ss); ss = __builtin_fmaf(c, c, ss);
      }
      sv[j] = s; qv[j] = ss;
    }
    auto s32 = [&](float a, float c) { auto r = __builtin_amdgcn_permlane32_swap(asu(a), asu(c), false, false); return asf(r[0]) + asf(r[1]); };
    auto s16 = [&](float a, float c) { auto r = __builtin_amdgcn_permlane16_swap(asu(a), asu(c), false, false); return asf(r[0]) + asf(r[1]); };
    const float A0 = s32(sv[0], qv[0]), A1 = s32(sv[1], qv[1]), A2 = s32(sv[2], qv[2]);
    float B = s16(A0, A1), C = s16(A2, A2);
    auto ror = [&](float v, auto n) { return v + asf(__builtin_amdgcn_update_dpp(0, asu(v), 0x120 + n.v, 0xf, 0xf, false)); };
    B = ror(B, IC<8>{}); C = ror(C, IC<8>{}); B = ror(B, IC<4>{}); C = ror(C, IC<4>{}); B = ror(B, IC<2>{}); C = ror(C, IC<2>{}); B = ror(B, IC<1>{}); C = ror(C, IC<1>{});
    const auto rb = __builtin_amdgcn_permlane32_swap(asu(B), asu(B), false, false), rc = __builtin_amdgcn_permlane32_swap(asu(C), asu(C), false, false);
    const float s = lane < 32 ? asf(rb[0]) : asf(rc[0]), ss = lane < 32 ? asf(rb[1]) : asf(rc[1]);
    const float mu = s * (1.0f / D), rs = __builtin_amdgcn_rsqf(fmaxf(__builtin_fmaf(-mu, mu, ss * (1.0f / D)), 0.f) + 1e-5f);
    const int j = q < 2 ? w + 4 * q : 8;
    if (tok == 0 && q < 3) stsc1(rsW, ST1_OFF + (u32)(g * 4096 + (9 * m + j) * 16), v4u{asu(mu), tag32, asu(rs), tag32});
  }

  const int l32 = lane & 31, hh = lane >> 5;
  const int qp = (lane & 15) >> 2, pp = lane & 3;
  {
    f32x16 aq = {}, ak = {}, av = {};
    int xa[4], wa1;
#pragma unroll
    for (int s4 = 0; s4 < 4; ++s4) xa[s4] = (16 + 32 * w + l32) * 128 + (((2 * s4 + hh) ^ ((l32 >> 1) & 7)) * 16);
    wa1 = P1_WOFF + (8 * hh + qp) * 192 + (q & 1) * 32 + pp * 8;
    constexpr float SC = 0.25f * 1.4426950408889634f, NEG = -3.0e38f;
    float bias0[4], bias1[4];
#pragma unroll
    for (int r = 0; r < 4; ++r) {
      const int d0 = tok + 16 - (4 * q + r), d1 = tok - (4 * q + r);
      bias0[r] = asf(opq(((d0 & 1) == 0 && d0 <= 14) ? 0 : (int)asu(NEG)));
      bias1[r] = asf(opq(((d1 & 1) == 0 && d1 >= 0 && d1 <= 14) ? 0 : (int)asu(NEG)));
    }
    struct P1F { bf16x8 xf; bf16x4 w[3][2]; };
    auto f_rd = [&](P1F& f, int u, int i) {
      const int sb = ((u >> 2) % 5) * P1_STAGE, s4 = u & 3;
      if (i == 0) f.xf = ldsb(xa[s4] + sb);
      else f.w[(i - 1) >> 1][(i - 1) & 1] = trr(wa1 + sb + s4 * 3072 + ((i - 1) >> 1) * 64 + ((i - 1) & 1) * 768);
    };
    P1F fa, fb;
    v4u st1;
    auto p1_step = [&](auto uc, P1F& f, P1F& n) {
      constexpr int u = uc.v;
      if constexpr ((u & 3) == 3 && u < 28) { WAITL0(); BAR(); }
      if constexpr (u == 24) ldsc1<0>(st1, rsW, ST1_OFF + (u32)(g * 4096 + (16 + 32 * w + l32) * 16), 0);
      sfor<0, 3>([&](auto ic) {
        constexpr int i = ic.v;
        const bf16x8 wf = cat(f.w[i][0], f.w[i][1]);
        if constexpr (i == 0) aq = mfma32(wf, f.xf, aq);
        if constexpr (i == 1) ak = mfma32(wf, f.xf, ak);
        if constexpr (i == 2) av = mfma32(f.xf, wf, av);
        if constexpr (u < 31) {
          if constexpr (i == 0) { f_rd(n, u + 1, 0); f_rd(n, u + 1, 1); f_rd(n, u + 1, 2); }
          if constexpr (i == 1) { f_rd(n, u + 1, 3); f_rd(n, u + 1, 4); }
          if constexpr (i == 2) { f_rd(n, u + 1, 5); f_rd(n, u + 1, 6); }
        }
        if constexpr (u == 29 && i == 2) {
          asm volatile("s_waitcnt vmcnt(0)" : "+v"(st1), "+v"(xres[0]), "+v"(xres[1]), "+v"(xres[2]), "+v"(xres[3])::"memory");
        }
        SCHED();
      });
    };
    BAR();
    sfor<0, 7>([&](auto i) { f_rd(fa, 0, i.v); });
    sfor<0, 15>([&](auto k) { p1_step(IC<2 * k.v>{}, fa, fb); p1_step(IC<2 * k.v + 1>{}, fb, fa); });
    for (u32 spins = 0; spins < (1u << 20); ++spins) {
      if (__all(st1.y == tag32 && st1.w == tag32)) break;
      __builtin_amdgcn_s_sleep(1);
      ldsc1<0>(st1, rsW, ST1_OFF + (u32)(g * 4096 + (16 + 32 * w + l32) * 16), 0);
      wait_for<0>(st1);
    }
    const float mean = asf(st1.x), rstd = asf(st1.z);
    LAS float* cs_ = lp<float>(M_CS);
    LAS float* mr = lp<float>(M_MR) + w * 64;
    if (lane < 32) { mr[lane] = mean; mr[32 + lane] = rstd; }
    p1_step(IC<30>{}, fa, fb);
    f32x4 mv[4], rv[4];
#pragma unroll
    for (int gg = 0; gg < 4; ++gg) { mv[gg] = *(LAS f32x4*)(mr + 8 * gg + 4 * hh); rv[gg] = *(LAS f32x4*)(mr + 32 + 8 * gg + 4 * hh); }
    p1_step(IC<31>{}, fb, fa);
    BAR();
    f32x4 cq[4], ck[4];
#pragma unroll
    for (int gg = 0; gg < 4; ++gg) { cq[gg] = *(LAS f32x4*)(cs_ + 8 * gg + 4 * hh); ck[gg] = *(LAS f32x4*)(cs_ + 32 + 8 * gg + 4 * hh); }
    const float csv = cs_[64 + l32];
    SCHED();
    v2u qp_[2][2], kp[2][2], vp[2][2];
    {
      u32 pq[8], pk[8], pv[8];
#pragma unroll
      for (int gg = 0; gg < 4; ++gg) {
        float a[4], k4[4], v4[4];
#pragma unroll
        for (int r = 0; r < 4; ++r) {
          a[r] = __builtin_fmaf(-mean, cq[gg][r], aq[4 * gg + r]);
          k4[r] = rstd * __builtin_fmaf(-mean, ck[gg][r], ak[4 * gg + r]);
          v4[r] = rv[gg][r] * __builtin_fmaf(-mv[gg][r], csv, av[4 * gg + r]);
        }
        pq[2 * gg] = pack2(a[0], a[1]); pq[2 * gg + 1] = pack2(a[2], a[3]);
        pk[2 * gg] = pack2(k4[0], k4[1]); pk[2 * gg + 1] = pack2(k4[2], k4[3]);
        pv[2 * gg] = pack2(v4[0], v4[1]); pv[2 * gg + 1] = pack2(v4[2], v4[3]);
      }
#pragma unroll
      for (int x = 0; x < 2; ++x) {
#pragma unroll
        for (int d = 0; d < 2; ++d) {
          const auto sq_ = __builtin_amdgcn_permlane16_swap(pq[4 * x + d], pq[4 * x + 2 + d], false, false);
          qp_[0][x][d] = sq_[0]; qp_[1][x][d] = sq_[1];
          const auto sk_ = __builtin_amdgcn_permlane16_swap(pk[4 * x + d], pk[4 * x + 2 + d], false, false);
          kp[0][x][d] = sk_[0]; kp[1][x][d] = sk_[1];
          const auto s1 = __builtin_amdgcn_permlane32_swap(pv[4 * x + d], pv[4 * x + 2 + d], false, false);
          const auto s2 = __builtin_amdgcn_permlane16_swap(s1[0], s1[1], false, false);
          vp[x][0][d] = s2[0]; vp[x][1][d] = s2[1];
        }
      }
    }
    {
#pragma unroll
      for (int hd = 0; hd < 2; ++hd) {
        *lp<v2u>(KV_OFF + w * 2048 + hd * 512 + lane * 8) = kp[1][hd];
        *lp<v2u>(KV_OFF + w * 2048 + (2 + hd) * 512 + lane * 8) = vp[1][hd];
      }
    }
    WAITL0();
    BAR();
    v2u kprev[2], vprev[2];
    {
      const int pb = w == 0 ? KVH_OFF : KV_OFF + (w - 1) * 2048;
#pragma unroll
      for (int hd = 0; hd < 2; ++hd) {
        kprev[hd] = *lp<v2u>(pb + hd * 512 + lane * 8);
        vprev[hd] = *lp<v2u>(pb + (2 + hd) * 512 + lane * 8);
      }
    }
    const auto rq = __builtin_amdgcn_permlane16_swap(asu(rstd), asu(rstd), false, false);
    const v2u ones2 = {0x3F803F80u, 0x3F803F80u};
    auto attend = [&](int t, int hd, v2u kA, v2u vA, bool prev_ok) {
      const float sc = SC * asf(rq[t]);
      f32x4 s0 = mfma16k16(kA, qp_[t][hd], f32x4{});
      f32x4 s1 = mfma16k16(kp[t][hd], qp_[t][hd], f32x4{});
      float e[8];
#pragma unroll
      for (int r = 0; r < 4; ++r) {
        e[r] = __builtin_fmaf(s0[r], sc, prev_ok ? bias0[r] : NEG);
        e[4 + r] = __builtin_fmaf(s1[r], sc, bias1[r]);
      }
      float mx = fmaxf(fmaxf(fmaxf(e[0], e[1]), fmaxf(e[2], e[3])), fmaxf(fmaxf(e[4], e[5]), fmaxf(e[6], e[7])));
      mx = xmax32(xmax16(mx));
#pragma unroll
      for (int r = 0; r < 8; ++r) e[r] = __builtin_amdgcn_exp2f(e[r] - mx);
      const v2u p0 = {pack2(e[0], e[1]), pack2(e[2], e[3])}, p1 = {pack2(e[4], e[5]), pack2(e[6], e[7])};
      f32x4 sm = mfma16k16(ones2, p0, f32x4{});
      sm = mfma16k16(ones2, p1, sm);
      f32x4 o = mfma16k16(vA, p0, f32x4{});
      o = mfma16k16(vp[t][hd], p1, o);
      const float inv = __builtin_amdgcn_rcpf(sm[0]);
      return v2u{pack2(o[0] * inv, o[1] * inv), pack2(o[2] * inv, o[3] * inv)};
    };
    {
      const bool ok = T0 + 32 * w > 0;
      const v2u o10 = attend(1, 0, kp[0][0], vp[0][0], true), o11 = attend(1, 1, kp[0][1], vp[0][1], true);
      const v2u o00 = attend(0, 0, kprev[0], vprev[0], ok), o01 = attend(0, 1, kprev[1], vprev[1], ok);
      stsc1(rsW, O_OFF + (u32)(((g * 8 + 2 * w + 1) * 16 + m) * 1024 + lane * 16), v4u{o10.x, o10.y, o11.x, o11.y});
      stsc1(rsW, O_OFF + (u32)(((g * 8 + 2 * w) * 16 + m) * 1024 + lane * 16), v4u{o00.x, o00.y, o01.x, o01.y});
    }
  }
  WAITVN(0);
  publish(fl, m, tag);

  {
    bf16x8 w2[8][4];
    int a[4];
    {
      const int row = 4 * q + qp, f = ((row >> 1) & 3) << 1;
#pragma unroll
      for (int n = 0; n < 4; ++n) a[n] = opq(W2_OFF + row * 128 + (((2 * n + (pp >> 1)) ^ f) * 16) + (pp & 1) * 8);
#pragma unroll
      for (int s = 0; s < 8; ++s)
#pragma unroll
        for (int n = 0; n < 4; ++n) w2[s][n] = tr2(a[n] + s * 4096, 2048);
    }
    poll(fl, m, 16, tag);
    BAR();
    v4u of[16];
    const u32 ob = O_OFF + (u32)((g * 8 + 4 * th + w) * 16) * 1024;
    sfor<0, 16>([&](auto i) { ldsc1<0>(of[i.v], rsW, lane * 16, ob + (u32)((i.v + 2 * ct) & 15) * 1024); });
    f32x4 acc[4] = {};
    sfor<0, 16>([&](auto sc) {
      constexpr int s = sc.v;
      wait_for<15 - s>(of[s]);
      const bf16x8 bfr = __builtin_bit_cast(bf16x8, of[s]);
#pragma unroll
      for (int n = 0; n < 4; ++n) {
        acc[n] = mfma16(w2[s & 7][n], bfr, acc[n]);
        if constexpr (s < 8) w2[s][n] = tr2(a[n] + (s + 8) * 4096, 2048);
      }
      SCHED();
    });
    float s = 0.f, ss = 0.f;
    const int tl = 16 * w + tok;
    const int t32 = g * 4 + 2 * th + (w >> 1);
    const u32 hoff = H_OFF + (u32)((t32 * 32 + 4 * ct) * 1024 + (16 * (w & 1) + tok + 32 * (q >> 1)) * 16 + (q & 1) * 8);
#pragma unroll
    for (int n = 0; n < 4; ++n) {
      const u32 h01 = pack2(bflo(xres[n].x) + acc[n][0], bfhi(xres[n].x) + acc[n][1]);
      const u32 h23 = pack2(bflo(xres[n].y) + acc[n][2], bfhi(xres[n].y) + acc[n][3]);
      const v2u hp = {h01, h23};
      stsc1_8(rsW, hoff + n * 1024, hp);
      *lp<v2u>(M_HK + tl * HK_PITCH + (16 * n + 4 * q) * 2) = hp;
      const float a0 = bflo(h01), a1 = bfhi(h01), a2 = bflo(h23), a3 = bfhi(h23);
      s += (a0 + a1) + (a2 + a3);
      ss = __builtin_fmaf(a0, a0, ss); ss = __builtin_fmaf(a1, a1, ss); ss = __builtin_fmaf(a2, a2, ss); ss = __builtin_fmaf(a3, a3, ss);
    }
    s = xadd32(xadd16(s)); ss = xadd32(xadd16(ss));
    if (q == 0) stsc1_8(rsW, ST_OFF + (u32)((ct * S + T0 + 64 * th + tl) * 8), v2u{asu(s), asu(ss)});
  }
  WAITVN(0);
  WAITL0();
  publish(fl + 256, m, tag);

  {
    const int wa = (8 * hh + qp) * 192 + (q & 1) * 32 + pp * 8;
    bf16x8 wA[6], wB[6];
    auto w3_read1 = [&](bf16x8& dst, int base, int i) { const int o = base + (i & 1) * 12288 + (i >> 1) * 64; dst = tr2(o, 768); };
    poll(fl + 256, m, 16, tag);
    BAR();
    const int t32 = g * 4 + w;
    v2u st[4];
#pragma unroll
    for (int i = 0; i < 4; ++i) ldsc1_8(st[i], rsW, (u32)(((hh * 4 + i) * S + l32) * 8), ST_OFF + (u32)(t32 * 32) * 8);
    v4u hf[16];
    const u32 hb = H_OFF + (u32)(t32 * 32) * 1024;
    auto ldh = [&](auto i) { ldsc1<(i.v & 3) * 1024>(hf[i.v & 15], rsW, lane * 16, hb + (u32)(((i.v >> 2) + m) & 7) * 4096); };
    sfor<0, 16>(ldh);
#pragma unroll
    for (int i = 0; i < 6; ++i) w3_read1(wA[i], wa, i);
    asm volatile("s_waitcnt vmcnt(16)" : "+v"(st[0]), "+v"(st[1]), "+v"(st[2]), "+v"(st[3])::"memory");
    float rs, nb;
    {
      float s = (asf(st[0].x) + asf(st[1].x)) + (asf(st[2].x) + asf(st[3].x)), ss = (asf(st[0].y) + asf(st[1].y)) + (asf(st[2].y) + asf(st[3].y));
      s = xadd32(s); ss = xadd32(ss);
      const float mu = s * (1.0f / D);
      rs = __builtin_amdgcn_rsqf(fmaxf(__builtin_fmaf(-mu, mu, ss * (1.0f / D)), 0.f) + 1e-5f);
      nb = -mu * rs;
    }
    auto ln = [&](v4u h, int d) {
      const f32x2 v = {bflo(h[d]), bfhi(h[d])};
      const f32x2 r = __builtin_elementwise_fma(v, f32x2{rs, rs}, f32x2{nb, nb});
      return pack2(r[0], r[1]);
    };
    v4u yA, yB;
    wait_for<15>(hf[0]);
#pragma unroll
    for (int d = 0; d < 4; ++d) yA[d] = ln(hf[0], d);
    ldh(IC<16>{});
    f32x16 acc[6] = {};
    auto step = [&](auto sc, bf16x8(&wc)[6], bf16x8(&wn)[6], v4u& yc, v4u& yn) {
      constexpr int s = sc.v, c1 = (s + 1) >> 2;
      if constexpr (s + 1 < 32) wait_for<(16 + s < 31 ? 16 + s : 31) - (s + 1)>(hf[(s + 1) & 15]);
      if constexpr (s >= 4 && (s & 3) == 0) BAR();
      const int base = opq(wa + (c1 % 6) * SLOT + (16 * ((s + 1) & 3)) * 192);
      const bf16x8 yb = __builtin_bit_cast(bf16x8, yc);
      __builtin_amdgcn_s_waitcnt(0xC07F);
      sfor<0, 6>([&](auto ic) {
        constexpr int i = ic.v;
        acc[i] = mfma32(wc[i], yb, acc[i]);
        if constexpr (s + 1 < 32) {
          if constexpr (i < 3) { w3_read1(wn[2 * i], base, 2 * i); w3_read1(wn[2 * i + 1], base, 2 * i + 1); }
          if constexpr (i >= 2) yn[i - 2] = ln(hf[(s + 1) & 15], i - 2);
        }
        if constexpr (i == 5 && 17 + s < 32) ldh(IC<(17 + s) & 31>{});
        SCHED();
      });
    };
    sfor<0, 14>([&](auto k) { step(IC<2 * k.v>{}, wA, wB, yA, yB); step(IC<2 * k.v + 1>{}, wB, wA, yB, yA); });
    {
      BAR();
      v4u y4[4];
      y4[0] = yA;
      sfor<1, 4>([&](auto j) {
        wait_for<3 - j.v>(hf[(28 + j.v) & 15]);
#pragma unroll
        for (int d = 0; d < 4; ++d) y4[j.v][d] = ln(hf[(28 + j.v) & 15], d);
      });
      const int base = opq(wa + (7 % 6) * SLOT);
      auto epi = [&](int i, int s) {
        v4u o;
#pragma unroll
        for (int d = 0; d < 4; ++d) {
          const int r = 8 * s + 2 * d;
          o[d] = pack2(fmaxf(acc[2 * i][r], 0.f) * acc[2 * i + 1][r], fmaxf(acc[2 * i][r + 1], 0.f) * acc[2 * i + 1][r + 1]);
        }
        const int f0 = 96 * m + 32 * i;
        if (f0 < F) stsc1(rsW, A_OFF + (u32)((t32 * 86 + f0 / 16 + s) * 1024 + lane * 16), o);
      };
      bf16x8 wl[3][8];
      auto rd_pair = [&](int i) {
#pragma unroll
        for (int j = 0; j < 4; ++j) { w3_read1(wl[i][2 * j], base + 16 * j * 192, 2 * i); w3_read1(wl[i][2 * j + 1], base + 16 * j * 192, 2 * i + 1); }
      };
      rd_pair(0); rd_pair(1);
      SCHED();
      sfor<0, 3>([&](auto ic) {
        constexpr int i = ic.v;
        if constexpr (i == 0) rd_pair(2);
        sfor<0, 4>([&](auto jc) {
          constexpr int j = jc.v;
          const bf16x8 yb = __builtin_bit_cast(bf16x8, y4[j]);
          acc[2 * i] = mfma32(wl[i][2 * j], yb, acc[2 * i]);
          acc[2 * i + 1] = mfma32(wl[i][2 * j + 1], yb, acc[2 * i + 1]);
          if constexpr (i > 0 && j == 1) epi(i - 1, 0);
          if constexpr (i > 0 && j == 3) epi(i - 1, 1);
          SCHED();
        });
      });
      BAR();
      epi(2, 0); epi(2, 1);
    }
    WAITVN(0);
  }
  publish(fl + 512, m, tag);

  {
    poll(fl + 512, m, 15, tag);
    BAR();
    const int mh = w & 1, kh = w >> 1;
    const int t32 = g * 4 + 2 * th + mh;
    v4u af[36];
    const u32 ab = A_OFF + (u32)(t32 * 86 + kh * 43) * 1024;
    auto lda = [&](auto i) { const int ps = i.v + 5 * ct; ldsc1<0>(af[i.v % 36], rsW, lane * 16, ab + (u32)(ps >= 43 ? ps - 43 : ps) * 1024); };
    sfor<0, 14>(lda);
    f32x16 acc[2] = {};
    int wa[2];
#pragma unroll
    for (int n = 0; n < 2; ++n) wa[n] = (4 * hh + qp) * 128 + ((n ^ ((qp >> 1) & 1)) * 64) + (q & 1) * 32 + pp * 8;
    bf16x8 wA[2], wB[2];
    auto w4_read = [&](bf16x8(&dst)[2], auto sc) {
      constexpr int s = sc.v;
      const int off = s < 42 ? ((s / 6 + 2) % 6) * SLOT + kh * 12288 + (s % 6) * 2048 : M_W4T + kh * 2048;
#pragma unroll
      for (int n = 0; n < 2; ++n) dst[n] = tr2(opq(wa[n] + off), 1024);
    };
    w4_read(wA, IC<0>{});
    auto step = [&](auto sc, bf16x8(&wc)[2], bf16x8(&wn)[2]) {
      constexpr int s = sc.v;
      constexpr int issued = (14 + 2 * (s < 8 ? s : 8) + (s > 8 ? s - 8 : 0)) < 43 ? (14 + 2 * (s < 8 ? s : 8) + (s > 8 ? s - 8 : 0)) : 43;
      wait_for<issued - 1 - s>(af[s % 36]);
      if constexpr (s == 6 || s == 30) BAR();
      if constexpr (s + 1 < 43) w4_read(wn, IC<s + 1>{});
      const bf16x8 bfr = __builtin_bit_cast(bf16x8, af[s % 36]);
      acc[0] = mfma32(wc[0], bfr, acc[0]);
      if constexpr (issued < 43) lda(IC<(issued < 43 ? issued : 0)>{});
      SCHED();
      acc[1] = mfma32(wc[1], bfr, acc[1]);
      if constexpr (s < 8 && issued + 1 < 43) lda(IC<(issued + 1 < 43 ? issued + 1 : 0)>{});
      SCHED();
    };
    sfor<0, 21>([&](auto k) { step(IC<2 * k.v>{}, wA, wB); step(IC<2 * k.v + 1>{}, wB, wA); });
    step(IC<42>{}, wA, wB);
    LAS f32x4* red = lp<f32x4>(3 * SLOT);
    {
      const f32x16 snd = kh ? acc[0] : acc[1];
#pragma unroll
      for (int gg = 0; gg < 4; ++gg) red[(w * 4 + gg) * 64 + lane] = f32x4{snd[4 * gg], snd[4 * gg + 1], snd[4 * gg + 2], snd[4 * gg + 3]};
    }
    if (m == 0 && tid == 0) __hip_atomic_store(ep, tag, RLX_AGENT);
    WAITL0(); BAR();
    {
      const f32x16 keep = kh ? acc[1] : acc[0];
      const int pw = w ^ 2;
      const rsrc_t rsO = mkrs(p.out, S * D * 2);
      v2u r[4];
#pragma unroll
      for (int gg = 0; gg < 4; ++gg) {
        const f32x4 o = red[(pw * 4 + gg) * 64 + lane];
        const v2u hk = *lp<v2u>(M_HK + (32 * mh + l32) * HK_PITCH + (32 * kh + 8 * gg + 4 * hh) * 2);
        r[gg].x = pack2(bflo(hk.x) + (keep[4 * gg] + o[0]), bfhi(hk.x) + (keep[4 * gg + 1] + o[1]));
        r[gg].y = pack2(bflo(hk.y) + (keep[4 * gg + 2] + o[2]), bfhi(hk.y) + (keep[4 * gg + 3] + o[3]));
      }
      const u32 ob = (u32)(T0 + 64 * th + 32 * mh + l32) * (D * 2) + (u32)(64 * ct + 32 * kh + 8 * hh) * 2;
#pragma unroll
      for (int gp = 0; gp < 2; ++gp) {
        const auto sx = __builtin_amdgcn_permlane32_swap(r[2 * gp].x, r[2 * gp + 1].x, false, false);
        const auto sy = __builtin_amdgcn_permlane32_swap(r[2 * gp].y, r[2 * gp + 1].y, false, false);
        __builtin_amdgcn_raw_buffer_store_b128(v4u{sx[0], sy[0], sx[1], sy[1]}, rsO, ob + gp * 32, 0, 0);
      }
    }
  }
}

extern "C" void kernel_launch(void* const* d_in, const int* in_sizes, int n_in, void* d_out, int out_size,
                              void* d_ws, size_t ws_size, hipStream_t stream) {
  Params p;
  p.x = (const u16*)d_in[0]; p.wqkv = (const u16*)d_in[1]; p.wo = (const u16*)d_in[2];
  p.wg = (const u16*)d_in[4]; p.wu = (const u16*)d_in[5]; p.wd = (const u16*)d_in[6];
  p.out = (u16*)d_out; p.ws = (unsigned char*)d_ws;
  (void)hipFuncSetAttribute((const void*)fused_block, hipFuncAttributeMaxDynamicSharedMemorySize, LDS_BYTES);
  fused_block<<<256, 512, LDS_BYTES, stream>>>(p);
}
```

```cpp
#include <hip/hip_runtime.h>
#include <stdint.h>

typedef unsigned short u16;
typedef unsigned u32;
typedef unsigned long long u64;
typedef short bf16x8 __attribute__((ext_vector_type(8)));
typedef short bf16x4 __attribute__((ext_vector_type(4)));
typedef float f32x2 __attribute__((ext_vector_type(2)));
typedef float f32x4 __attribute__((ext_vector_type(4)));
typedef float f32x16 __attribute__((ext_vector_type(16)));
typedef unsigned v4u __attribute__((ext_vector_type(4)));
typedef unsigned v2u __attribute__((ext_vector_type(2)));
typedef __bf16 bf2_t __attribute__((ext_vector_type(2)));
#define GAS __attribute__((address_space(1)))
#define LAS __attribute__((address_space(3)))
#define DI __device__ __forceinline__
#define RLX_AGENT __ATOMIC_RELAXED, __HIP_MEMORY_SCOPE_AGENT
typedef GAS u64 gu64;
typedef __amdgpu_buffer_rsrc_t rsrc_t;

constexpr int S = 2048, D = 512, F = 1376;
constexpr u32 O_OFF = 0;
constexpr u32 H_OFF = 2u << 20;
constexpr u32 A_OFF = 4u << 20;
constexpr u32 ST_OFF = 10u << 20;
constexpr u32 ST1_OFF = (10u << 20) + (512u << 10);
constexpr u32 FL_OFF = 11u << 20;
constexpr u32 EP_OFF = 12u << 20;
constexpr u32 WS_BYTES = 13u << 20;

constexpr int SLOT = 24576;
constexpr int MISC = 6 * SLOT;
constexpr int LDS_BYTES = 163840;
constexpr int P1_STAGE = 30720;
constexpr int P1_WOFF = 18432;
constexpr int W2_OFF = 3 * P1_STAGE;
constexpr int M_CS = 157696;
constexpr int M_MR = M_CS + 1024;
constexpr int KVH_OFF = M_CS + 2048;
constexpr int KV_OFF = 3 * SLOT;
constexpr int M_HK = MISC;
constexpr int M_W4T = MISC + 8704;
constexpr int HK_PITCH = 136;

struct Params {
  const u16* x; const u16* wqkv; const u16* wo; const u16* wg; const u16* wu; const u16* wd;
  u16* out; unsigned char* ws;
};

template <int I> struct IC { static constexpr int v = I; };
template <int I, int N, class Fn> DI void sfor(Fn&& f) { if constexpr (I < N) { f(IC<I>{}); sfor<I + 1, N>(f); } }

DI u32 pack2(float a, float b) { f32x2 v = {a, b}; bf2_t r = __builtin_convertvector(v, bf2_t); return __builtin_bit_cast(u32, r); }
DI float bflo(u32 u) { return __builtin_bit_cast(float, u << 16); }
DI float bfhi(u32 u) { return __builtin_bit_cast(float, u & 0xffff0000u); }
DI float asf(u32 u) { return __builtin_bit_cast(float, u); }
DI u32 asu(float f) { return __builtin_bit_cast(u32, f); }
DI f32x4 mfma16(bf16x8 a, bf16x8 b, f32x4 c) { return __builtin_amdgcn_mfma_f32_16x16x32_bf16(a, b, c, 0, 0, 0); }
DI f32x4 mfma16k16(v2u a, v2u b, f32x4 c) { return __builtin_amdgcn_mfma_f32_16x16x16bf16_1k(__builtin_bit_cast(bf16x4, a), __builtin_bit_cast(bf16x4, b), c, 0, 0, 0); }
DI f32x16 mfma32(bf16x8 a, bf16x8 b, f32x16 c) { return __builtin_amdgcn_mfma_f32_32x32x16_bf16(a, b, c, 0, 0, 0); }
DI bf16x8 cat(bf16x4 a, bf16x4 b) { return __builtin_shufflevector(a, b, 0, 1, 2, 3, 4, 5, 6, 7); }
#pragma clang diagnostic ignored "-Wint-to-pointer-cast"
DI bf16x4 trr(int off) { return __builtin_amdgcn_ds_read_tr16_b64_v4i16((LAS bf16x4*)(u32)off); }
DI bf16x8 tr2(int off, int d) { return cat(trr(off), trr(off + d)); }
DI bf16x8 ldsb(int off) { return *(LAS bf16x8*)(u32)off; }
template <class T> DI LAS T* lp(int off) { return (LAS T*)(u32)off; }
DI rsrc_t mkrs(const void* p, u32 bytes) { return __builtin_amdgcn_make_buffer_rsrc((void*)p, 0, bytes, 0x00020000); }
DI u32 rfl(u32 v) { return __builtin_amdgcn_readfirstlane(v); }
DI int opq(int v) { asm volatile("" : "+v"(v)); return v; }
DI float xmax16(float v) { auto r = __builtin_amdgcn_permlane16_swap(asu(v), asu(v), false, false); return fmaxf(asf(r[0]), asf(r[1])); }
DI float xmax32(float v) { auto r = __builtin_amdgcn_permlane32_swap(asu(v), asu(v), false, false); return fmaxf(asf(r[0]), asf(r[1])); }
DI float xadd16(float v) { auto r = __builtin_amdgcn_permlane16_swap(asu(v), asu(v), false, false); return asf(r[0]) + asf(r[1]); }
DI float xadd32(float v) { auto r = __builtin_amdgcn_permlane32_swap(asu(v), asu(v), false, false); return asf(r[0]) + asf(r[1]); }
#define BAR() __builtin_amdgcn_s_barrier()
#define SCHED() __builtin_amdgcn_sched_barrier(0)
#define WAITVN(n) asm volatile("s_waitcnt vmcnt(%0)" ::"i"(n) : "memory")
#define WAITL0() asm volatile("s_waitcnt lgkmcnt(0)" ::: "memory")

DI void dma(rsrc_t rs, u32 voff, u32 soff, u32 ldsaddr) {
  asm volatile("s_mov_b32 m0, %0\n\ts_nop 0\n\tbuffer_load_dwordx4 %1, %2, %3 offen lds" ::"s"(ldsaddr), "v"(voff), "s"(rs), "s"(soff) : "memory");
}
template <int IMM> DI void ldsc1(v4u& dst, rsrc_t rs, u32 voff, u32 soff) {
  asm volatile("buffer_load_dwordx4 %0, %1, %2, %3 offen offset:%4 sc1" : "=v"(dst) : "v"(voff), "s"(rs), "s"(soff), "i"(IMM) : "memory");
}
DI void ldsc1_8(v2u& dst, rsrc_t rs, u32 voff, u32 soff) {
  asm volatile("buffer_load_dwordx2 %0, %1, %2, %3 offen sc1" : "=v"(dst) : "v"(voff), "s"(rs), "s"(soff) : "memory");
}
DI void ld16(v4u& dst, rsrc_t rs, u32 voff, u32 soff) {
  asm volatile("buffer_load_dwordx4 %0, %1, %2, %3 offen" : "=v"(dst) : "v"(voff), "s"(rs), "s"(soff) : "memory");
}
DI void ld4_keep(u32& dst, rsrc_t rs, u32 voff) { asm volatile("buffer_load_dword %0, %1, %2, 0 offen" : "+v"(dst) : "v"(voff), "s"(rs) : "memory"); }
template <int IMM> DI void ld8(v2u& dst, rsrc_t rs, u32 voff, u32 soff) {
  asm volatile("buffer_load_dwordx2 %0, %1, %2, %3 offen offset:%4" : "=v"(dst) : "v"(voff), "s"(rs), "s"(soff), "i"(IMM) : "memory");
}
template <int N> DI void wait_for(v4u& a) { asm volatile("s_waitcnt vmcnt(%1)" : "+v"(a) : "i"(N > 63 ? 63 : N) : "memory"); }
DI void stsc1(rsrc_t rs, u32 off, v4u v) { __builtin_amdgcn_raw_buffer_store_b128(v, rs, off, 0, 16); }
DI void stsc1_8(rsrc_t rs, u32 off, v2u v) { __builtin_amdgcn_raw_buffer_store_b64(v, rs, off, 0, 16); }

#define STAMP(i) do {} while (0)

DI void publish(gu64* fl, int m, u64 tag) {
  BAR();
  if (threadIdx.x < 16) __hip_atomic_store(fl + threadIdx.x * 16 + m, tag, RLX_AGENT);
}
DI void poll(gu64* fl, int m, int nprod, u64 tag) {
  if (threadIdx.x < 64) {
    const int lane = threadIdx.x;
    for (u32 spins = 0; spins < (1u << 20); ++spins) {
      u64 v = tag;
      if (lane < nprod) v = __hip_atomic_load(fl + m * 16 + lane, RLX_AGENT);
      if (__all(v == tag)) break;
    }
    __builtin_amdgcn_fence(__ATOMIC_ACQUIRE, "wavefront");
  }
}

__global__ __launch_bounds__(512, 2) void fused_block(Params p) {
  extern __shared__ __attribute__((aligned(16))) unsigned char smem[];
  const int tid = threadIdx.x, lane = tid & 63, w8 = (int)rfl(tid >> 6), w = w8 & 3;
  const int b = blockIdx.x, g = ((b & 7) >> 1) * 4 + ((b >> 3) >> 3), m = (b & 1) * 8 + ((b >> 3) & 7);
  const int T0 = g * 128;
  const int tok = lane & 15, q = lane >> 4;
  const int th = m >> 3, ct = m & 7;
  const rsrc_t rsW = mkrs(p.ws, WS_BYTES);
  const rsrc_t rsX = mkrs(p.x, S * D * 2);
  const u32 halo0 = (u32)(T0 >= 16 ? T0 - 16 : 0);
  asm volatile("" ::"s"((u32)(size_t)smem));

  if (w8 >= 4) {
    const rsrc_t rsW1 = mkrs(p.wqkv, D * 3 * D * 2);
    v2u epv;
    ldsc1_8(epv, rsW, 0, EP_OFF + g * 128);
    u32 vx0, vx1, vxh, vw0, vw1, vw2;
    {
      const int r8 = lane >> 3, sl = lane & 7;
      vx0 = r8 * 1024 + ((sl ^ ((lane >> 4) & 7)) * 16);
      vx1 = r8 * 1024 + ((sl ^ ((4 + (lane >> 4)) & 7)) * 16);
      vxh = (w & 1) ? vx1 : vx0;
      auto pat = [&](int j) { const int e = j * 64 + lane, r = e / 12, pc = e % 12; return (u32)(r * 3072 + (pc >> 2) * 1024 + (pc & 3) * 16); };
      vw0 = pat(0); vw1 = pat(1); vw2 = pat(2);
    }
    const u32 sxh = (halo0 + 8 * (w & 1)) * 1024, sxm = (u32)(T0 + 32 * w) * 1024, sw1 = (u32)(16 * w) * 3072 + m * 64;
    const int rot = (g + m) & 7;
    auto p1_piece = [&](int cc, int j) {
      const u32 lb = (cc % 5) * P1_STAGE;
      const u32 c = (u32)((cc + rot) & 7);
      if (j == 0) dma(rsX, vxh, sxh + c * 128, lb + (w & 1) * 1024);
      if (j == 1) dma(rsW1, vw0, sw1 + c * (64 * 3072), lb + P1_WOFF + (3 * w) * 1024);
      if (j == 2) dma(rsX, vx0, sxm + c * 128, lb + (2 + 4 * w) * 1024);
      if (j == 3) dma(rsW1, vw1, sw1 + c * (64 * 3072), lb + P1_WOFF + (3 * w + 1) * 1024);
      if (j == 4) dma(rsX, vx1, sxm + c * 128 + 8192, lb + (3 + 4 * w) * 1024);
      if (j == 5) dma(rsW1, vw2, sw1 + c * (64 * 3072), lb + P1_WOFF + (3 * w + 2) * 1024);
      if (j == 6) dma(rsX, vx0, sxm + c * 128 + 16384, lb + (4 + 4 * w) * 1024);
      if (j == 7) dma(rsX, vx1, sxm + c * 128 + 24576, lb + (5 + 4 * w) * 1024);
    };
    const rsrc_t rsW2 = mkrs(p.wo, D * D * 2), rsG = mkrs(p.wg, D * F * 2), rsU = mkrs(p.wu, D * F * 2), rsD = mkrs(p.wd, F * D * 2);
    const u32 v2 = (lane >> 3) * 1024 + (((lane & 7) ^ (((lane >> 4) & 3) << 1)) * 16);
    auto w2_piece = [&](int i) {
      const int ii = 16 * w + i, ls = ii >> 2;
      dma(rsW2, v2, (u32)(32 * (ls & 15) + 8 * (ii & 3)) * 1024 + ct * 128, W2_OFF + ii * 1024);
    };
    const int qp = (lane & 15) >> 2, pp = lane & 3;
    f32x4 ahA = {}, ahB = {};
    const int xha0 = tok * 128 + ((q ^ (tok >> 1)) * 16), xha1 = tok * 128 + (((4 + q) ^ (tok >> 1)) * 16);
    const int wha = P1_WOFF + (8 * q + qp) * 192 + (1 + (w >> 1)) * 64 + (w & 1) * 32 + pp * 8;
    const int whb = P1_WOFF + (8 * q + qp) * 192 + (w < 2 ? 0 : 128) + (w & 1) * 32 + pp * 8;
    auto halo_with = [&](int hc, auto&& issue) {
      const int sb = (hc % 5) * P1_STAGE;
      v4u xh[2]; bf16x8 fa[2], fb[2];
#pragma unroll
      for (int s2 = 0; s2 < 2; ++s2) {
        xh[s2] = __builtin_bit_cast(v4u, ldsb((s2 ? xha1 : xha0) + sb));
        fa[s2] = tr2(wha + sb + s2 * 6144, 768); fb[s2] = tr2(whb + sb + s2 * 6144, 768);
      }
      SCHED();
      issue(0); issue(1); issue(2); issue(3);
      SCHED();
#pragma unroll
      for (int s2 = 0; s2 < 2; ++s2) {
#pragma unroll
        for (int d = 0; d < 4; ++d) xh[s2][d] = tok == 0 ? 0x3F803F80u : xh[s2][d];
        const bf16x8 xhb = __builtin_bit_cast(bf16x8, xh[s2]);
        ahA = mfma16(fa[s2], xhb, ahA);
        ahB = mfma16(fb[s2], xhb, ahB);
      }
      SCHED();
      issue(4); issue(5); issue(6); issue(7);
    };
    sfor<0, 24>([&](auto i) { p1_piece(i.v >> 3, i.v & 7); });
    WAITVN(16); BAR();
    halo_with(0, [&](int j) { p1_piece(3, j); });
    WAITVN(16); BAR();
    halo_with(1, [&](int j) { p1_piece(4, j); });
    WAITVN(16); BAR();
    halo_with(2, [&](int j) { p1_piece(5, j); });
    WAITVN(16); BAR();
    halo_with(3, [&](int j) { p1_piece(6, j); });
    WAITVN(16); BAR();
    halo_with(4, [&](int j) { p1_piece(7, j); });
    u32 touch = 0;
    {
      const u32 wg_ = (u32)(b * 4 + w);
      const u32 o4 = (u32)lane < 4u ? (wg_ * 4u + lane) * 128u : 0xFFFFFF00u, o11 = (u32)lane < 11u ? (wg_ * 11u + lane) * 128u : 0xFFFFFF00u;
      ld4_keep(touch, rsW2, o4); ld4_keep(touch, rsG, o11); ld4_keep(touch, rsU, o11); ld4_keep(touch, rsD, o11);
    }
    v4u sth;
    ldsc1<0>(sth, rsW, ST1_OFF + (u32)(g * 4096 + tok * 16), 0);
    WAITVN(21); BAR();
    halo_with(5, [&](int) {});
    WAITVN(13); BAR();
    halo_with(6, [&](int) {});
    WAITVN(5); BAR();
    halo_with(7, [&](int j) { w2_piece(j); });
    asm volatile("s_waitcnt vmcnt(8)" : "+v"(sth), "+v"(touch), "+v"(epv)::"memory");
    const u64 tag = (((u64)rfl(epv.y) << 32) | rfl(epv.x)) + 1;
    const u32 tag32 = (u32)tag ^ (u32)(tag >> 32) ^ 0x9E3779B9u;
    for (u32 spins = 0; spins < (1u << 20); ++spins) {
      if (__all(sth.y == tag32 && sth.w == tag32)) break;
      __builtin_amdgcn_s_sleep(1);
      ldsc1<0>(sth, rsW, ST1_OFF + (u32)(g * 4096 + tok * 16), 0);
      wait_for<0>(sth);
    }
    {
      LAS float* cs_ = lp<float>(M_CS);
      if (tok == 0) {
        *(LAS f32x4*)(cs_ + 32 + 16 * w + 4 * q) = ahA;
        if (w < 2) *(LAS f32x4*)(cs_ + 16 * w + 4 * q) = ahB;
      }
      const float hmu = asf(sth.x), hrs = asf(sth.z);
      float hv[4];
#pragma unroll
      for (int r = 0; r < 4; ++r) {
        const float c = asf(__builtin_amdgcn_update_dpp(0, asu(ahA[r]), 0x150, 0xf, 0xf, false));
        hv[r] = hrs * __builtin_fmaf(-hmu, c, ahA[r]);
      }
      const u32 o0 = pack2(hv[0], hv[1]), o1 = pack2(hv[2], hv[3]);
      if (w < 2) *lp<v2u>(KVH_OFF + w * 512 + (tok + 16 * (((q & 1) << 1) | (q >> 1))) * 8) = v2u{o0, o1};
      else {
        LAS u16* vv = lp<u16>(KVH_OFF + w * 512) + (4 * q + 16 * (tok >> 2)) * 4 + (tok & 3);
        vv[0] = (u16)o0; vv[4] = (u16)(o0 >> 16); vv[8] = (u16)o1; vv[12] = (u16)(o1 >> 16);
      }
    }
    WAITL0();
    const u32 v30 = (lane / 12) * (F * 2) + (lane % 12) * 16, v31 = ((64 + lane) / 12) * (F * 2) + ((64 + lane) % 12) * 16,
              v32 = ((128 + lane) / 12) * (F * 2) + ((128 + lane) % 12) * 16;
    const u32 s3 = (u32)(16 * w) * (F * 2) + m * 192;
    auto w3_chunk = [&](int c) {
      const u32 so = s3 + (u32)(64 * (c & 7)) * (F * 2), lb = (c % 6) * SLOT + 3 * w * 1024;
      dma(rsG, v30, so, lb); dma(rsG, v31, so, lb + 1024); dma(rsG, v32, so, lb + 2048);
      dma(rsU, v30, so, lb + 12288); dma(rsU, v31, so, lb + 12288 + 1024); dma(rsU, v32, so, lb + 12288 + 2048);
    };
    const u32 v4s = (lane >> 3) * 1024 + (((lane & 7) ^ (((lane >> 4) & 1) << 2)) * 16);
    auto w4_row = [&](int ls) { const int ps = ls; return (u32)((ps >= 43 ? ps - 43 : ps) * 16); };
    auto w4_part = [&](int part) {
      const u32 lb = ((part + 2) % 6) * SLOT + 3 * w * 1024;
#pragma unroll
      for (int j = 0; j < 3; ++j) {
        const int jj = 3 * w + j;
        const u32 so = (w4_row(6 * part + (jj >> 1)) + (jj & 1) * 8) * 1024 + ct * 128;
        dma(rsD, v4s, so, lb + j * 1024); dma(rsD, v4s, so + 688 * 1024, lb + 12288 + j * 1024);
      }
    };
    BAR();
    sfor<8, 16>([&](auto i) { w2_piece(i.v); });
    BAR();
    w3_chunk(0); w3_chunk(1);
    WAITVN(12);
    BAR();
    w3_chunk(2);
    BAR();
    BAR();
    w3_chunk(3);
    WAITVN(6); BAR();
    w3_chunk(4); w3_chunk(5);
    BAR(); w3_chunk(6);
    WAITVN(18); BAR(); w3_chunk(7);
    WAITVN(18); BAR(); w4_part(0);
    WAITVN(18); BAR(); w4_part(1);
    WAITVN(18); BAR(); w4_part(2);
    WAITVN(18); BAR(); w4_part(3);
    BAR(); w4_part(4);
    BAR();
    w4_part(5);
    dma(rsD, v4s, ((u32)(w >> 1) * 688 + w4_row(42) + (w & 1) * 8) * 1024 + ct * 128, M_W4T + w * 1024);
    BAR();
    WAITVN(7); BAR();
    WAITVN(0); BAR(); w4_part(6);
    WAITVN(0); BAR();
    BAR();
    return;
  }

  __builtin_amdgcn_s_setprio(2);
  STAMP(0);
  gu64* ep = (gu64*)(p.ws + EP_OFF) + g * 16;
  gu64* fl = (gu64*)(p.ws + FL_OFF) + g * 768;
  v2u epv, xres[4];
  v4u srow[3];
  ldsc1_8(epv, rsW, 0, EP_OFF + g * 128);
  sfor<0, 3>([&](auto jc) {
    const int j = jc.v < 2 ? w + 4 * jc.v : 8, R = 9 * m + j;
    const u32 token = R < 16 ? halo0 + R : (u32)(T0 + R - 16);
    ld16(srow[jc.v], rsX, lane * 16, token * 1024);
  });
  {
    const u32 vo = (u32)tok * 1024 + q * 8, so = (u32)(T0 + 64 * th + 16 * w) * 1024 + ct * 128;
    ld8<0>(xres[0], rsX, vo, so); ld8<32>(xres[1], rsX, vo, so); ld8<64>(xres[2], rsX, vo, so); ld8<96>(xres[3], rsX, vo, so);
  }
  asm volatile("s_waitcnt vmcnt(4)" : "+v"(epv), "+v"(srow[0]), "+v"(srow[1]), "+v"(srow[2])::"memory");
  const u64 tag = (((u64)rfl(epv.y) << 32) | rfl(epv.x)) + 1;
  const u32 tag32 = (u32)tag ^ (u32)(tag >> 32) ^ 0x9E3779B9u;
    {
    float sv[3], qv[3];
#pragma unroll
    for (int j = 0; j < 3; ++j) {
      float s = 0.f, ss = 0.f;
#pragma unroll
      for (int d = 0; d < 4; ++d) {
        const float a = bflo(srow[j][d]), c = bfhi(srow[j][d]);
        s += a + c; ss = __builtin_fmaf(a, a, ss); ss = __builtin_fmaf(c, c, ss);
      }
      sv[j] = s; qv[j] = ss;
    }
    auto s32 = [&](float a, float c) { auto r = __builtin_amdgcn_permlane32_swap(asu(a), asu(c), false, false); return asf(r[0]) + asf(r[1]); };
    auto s16 = [&](float a, float c) { auto r = __builtin_amdgcn_permlane16_swap(asu(a), asu(c), false, false); return asf(r[0]) + asf(r[1]); };
    const float A0 = s32(sv[0], qv[0]), A1 = s32(sv[1], qv[1]), A2 = s32(sv[2], qv[2]);
    float B = s16(A0, A1), C = s16(A2, A2);
    auto ror = [&](float v, auto n) { return v + asf(__builtin_amdgcn_update_dpp(0, asu(v), 0x120 + n.v, 0xf, 0xf, false)); };
    B = ror(B, IC<8>{}); C = ror(C, IC<8>{}); B = ror(B, IC<4>{}); C = ror(C, IC<4>{}); B = ror(B, IC<2>{}); C = ror(C, IC<2>{}); B = ror(B, IC<1>{}); C = ror(C, IC<1>{});
    const auto rb = __builtin_amdgcn_permlane32_swap(asu(B), asu(B), false, false), rc = __builtin_amdgcn_permlane32_swap(asu(C), asu(C), false, false);
    const float s = lane < 32 ? asf(rb[0]) : asf(rc[0]), ss = lane < 32 ? asf(rb[1]) : asf(rc[1]);
    const float mu = s * (1.0f / D), rs = __builtin_amdgcn_rsqf(fmaxf(__builtin_fmaf(-mu, mu, ss * (1.0f / D)), 0.f) + 1e-5f);
    const int j = q < 2 ? w + 4 * q : 8;
    if (tok == 0 && q < 3) stsc1(rsW, ST1_OFF + (u32)(g * 4096 + (9 * m + j) * 16), v4u{asu(mu), tag32, asu(rs), tag32});
  }

  const int l32 = lane & 31, hh = lane >> 5;
  const int qp = (lane & 15) >> 2, pp = lane & 3;
  {
    f32x16 aq = {}, ak = {}, av = {};
    int xa[4], wa1;
#pragma unroll
    for (int s4 = 0; s4 < 4; ++s4) xa[s4] = (16 + 32 * w + l32) * 128 + (((2 * s4 + hh) ^ ((l32 >> 1) & 7)) * 16);
    wa1 = P1_WOFF + (8 * hh + qp) * 192 + (q & 1) * 32 + pp * 8;
    constexpr float SC = 0.25f * 1.4426950408889634f, NEG = -3.0e38f;
    float bias0[4], bias1[4];
#pragma unroll
    for (int r = 0; r < 4; ++r) {
      const int d0 = tok + 16 - (4 * q + r), d1 = tok - (4 * q + r);
      bias0[r] = asf(opq(((d0 & 1) == 0 && d0 <= 14) ? 0 : (int)asu(NEG)));
      bias1[r] = asf(opq(((d1 & 1) == 0 && d1 >= 0 && d1 <= 14) ? 0 : (int)asu(NEG)));
    }
    struct P1F { bf16x8 xf; bf16x4 w[3][2]; };
    auto f_rd = [&](P1F& f, int u, int i) {
      const int sb = ((u >> 2) % 5) * P1_STAGE, s4 = u & 3;
      if (i == 0) f.xf = ldsb(xa[s4] + sb);
      else f.w[(i - 1) >> 1][(i - 1) & 1] = trr(wa1 + sb + s4 * 3072 + ((i - 1) >> 1) * 64 + ((i - 1) & 1) * 768);
    };
    P1F fa, fb;
    v4u st1;
    auto p1_step = [&](auto uc, P1F& f, P1F& n) {
      constexpr int u = uc.v;
      if constexpr ((u & 3) == 3 && u < 28) { WAITL0(); BAR(); }
      if constexpr (u == 24) ldsc1<0>(st1, rsW, ST1_OFF + (u32)(g * 4096 + (16 + 32 * w + l32) * 16), 0);
      sfor<0, 3>([&](auto ic) {
        constexpr int i = ic.v;
        const bf16x8 wf = cat(f.w[i][0], f.w[i][1]);
        if constexpr (i == 0) aq = mfma32(wf, f.xf, aq);
        if constexpr (i == 1) ak = mfma32(wf, f.xf, ak);
        if constexpr (i == 2) av = mfma32(f.xf, wf, av);
        if constexpr (u < 31) {
          if constexpr (i == 0) { f_rd(n, u + 1, 0); f_rd(n, u + 1, 1); f_rd(n, u + 1, 2); }
          if constexpr (i == 1) { f_rd(n, u + 1, 3); f_rd(n, u + 1, 4); }
          if constexpr (i == 2) { f_rd(n, u + 1, 5); f_rd(n, u + 1, 6); }
        }
        if constexpr (u == 29 && i == 2) {
          asm volatile("s_waitcnt vmcnt(0)" : "+v"(st1), "+v"(xres[0]), "+v"(xres[1]), "+v"(xres[2]), "+v"(xres[3])::"memory");
        }
        SCHED();
      });
    };
    BAR();
    sfor<0, 7>([&](auto i) { f_rd(fa, 0, i.v); });
    sfor<0, 15>([&](auto k) { p1_step(IC<2 * k.v>{}, fa, fb); p1_step(IC<2 * k.v + 1>{}, fb, fa); });
    for (u32 spins = 0; spins < (1u << 20); ++spins) {
      if (__all(st1.y == tag32 && st1.w == tag32)) break;
      __builtin_amdgcn_s_sleep(1);
      ldsc1<0>(st1, rsW, ST1_OFF + (u32)(g * 4096 + (16 + 32 * w + l32) * 16), 0);
      wait_for<0>(st1);
    }
    const float mean = asf(st1.x), rstd = asf(st1.z);
    LAS float* cs_ = lp<float>(M_CS);
    LAS float* mr = lp<float>(M_MR) + w * 64;
    if (lane < 32) { mr[lane] = mean; mr[32 + lane] = rstd; }
    p1_step(IC<30>{}, fa, fb);
    f32x4 mv[4], rv[4];
#pragma unroll
    for (int gg = 0; gg < 4; ++gg) { mv[gg] = *(LAS f32x4*)(mr + 8 * gg + 4 * hh); rv[gg] = *(LAS f32x4*)(mr + 32 + 8 * gg + 4 * hh); }
    p1_step(IC<31>{}, fb, fa);
    STAMP(1);
    BAR();
    f32x4 cq[4], ck[4];
#pragma unroll
    for (int gg = 0; gg < 4; ++gg) { cq[gg] = *(LAS f32x4*)(cs_ + 8 * gg + 4 * hh); ck[gg] = *(LAS f32x4*)(cs_ + 32 + 8 * gg + 4 * hh); }
    const float csv = cs_[64 + l32];
    SCHED();
    v2u qp_[2][2], kp[2][2], vp[2][2];
    {
      u32 pq[8], pk[8], pv[8];
#pragma unroll
      for (int gg = 0; gg < 4; ++gg) {
        float a[4], k4[4], v4[4];
#pragma unroll
        for (int r = 0; r < 4; ++r) {
          a[r] = __builtin_fmaf(-mean, cq[gg][r], aq[4 * gg + r]);
          k4[r] = rstd * __builtin_fmaf(-mean, ck[gg][r], ak[4 * gg + r]);
          v4[r] = rv[gg][r] * __builtin_fmaf(-mv[gg][r], csv, av[4 * gg + r]);
        }
        pq[2 * gg] = pack2(a[0], a[1]); pq[2 * gg + 1] = pack2(a[2], a[3]);
        pk[2 * gg] = pack2(k4[0], k4[1]); pk[2 * gg + 1] = pack2(k4[2], k4[3]);
        pv[2 * gg] = pack2(v4[0], v4[1]); pv[2 * gg + 1] = pack2(v4[2], v4[3]);
      }
#pragma unroll
      for (int x = 0; x < 2; ++x) {
#pragma unroll
        for (int d = 0; d < 2; ++d) {
          const auto sq_ = __builtin_amdgcn_permlane16_swap(pq[4 * x + d], pq[4 * x + 2 + d], false, false);
          qp_[0][x][d] = sq_[0]; qp_[1][x][d] = sq_[1];
          const auto sk_ = __builtin_amdgcn_permlane16_swap(pk[4 * x + d], pk[4 * x + 2 + d], false, false);
          kp[0][x][d] = sk_[0]; kp[1][x][d] = sk_[1];
          const auto s1 = __builtin_amdgcn_permlane32_swap(pv[4 * x + d], pv[4 * x + 2 + d], false, false);
          const auto s2 = __builtin_amdgcn_permlane16_swap(s1[0], s1[1], false, false);
          vp[x][0][d] = s2[0]; vp[x][1][d] = s2[1];
        }
      }
    }
    {
#pragma unroll
      for (int hd = 0; hd < 2; ++hd) {
        *lp<v2u>(KV_OFF + w * 2048 + hd * 512 + lane * 8) = kp[1][hd];
        *lp<v2u>(KV_OFF + w * 2048 + (2 + hd) * 512 + lane * 8) = vp[1][hd];
      }
    }
    WAITL0();
    BAR();
    v2u kprev[2], vprev[2];
    {
      const int pb = w == 0 ? KVH_OFF : KV_OFF + (w - 1) * 2048;
#pragma unroll
      for (int hd = 0; hd < 2; ++hd) {
        kprev[hd] = *lp<v2u>(pb + hd * 512 + lane * 8);
        vprev[hd] = *lp<v2u>(pb + (2 + hd) * 512 + lane * 8);
      }
    }
    const auto rq = __builtin_amdgcn_permlane16_swap(asu(rstd), asu(rstd), false, false);
    const v2u ones2 = {0x3F803F80u, 0x3F803F80u};
    auto attend = [&](int t, int hd, v2u kA, v2u vA, bool prev_ok) {
      const float sc = SC * asf(rq[t]);
      f32x4 s0 = mfma16k16(kA, qp_[t][hd], f32x4{});
      f32x4 s1 = mfma16k16(kp[t][hd], qp_[t][hd], f32x4{});
      float e[8];
#pragma unroll
      for (int r = 0; r < 4; ++r) {
        e[r] = __builtin_fmaf(s0[r], sc, prev_ok ? bias0[r] : NEG);
        e[4 + r] = __builtin_fmaf(s1[r], sc, bias1[r]);
      }
      float mx = fmaxf(fmaxf(fmaxf(e[0], e[1]), fmaxf(e[2], e[3])), fmaxf(fmaxf(e[4], e[5]), fmaxf(e[6], e[7])));
      mx = xmax32(xmax16(mx));
#pragma unroll
      for (int r = 0; r < 8; ++r) e[r] = __builtin_amdgcn_exp2f(e[r] - mx);
      const v2u p0 = {pack2(e[0], e[1]), pack2(e[2], e[3])}, p1 = {pack2(e[4], e[5]), pack2(e[6], e[7])};
      f32x4 sm = mfma16k16(ones2, p0, f32x4{});
      sm = mfma16k16(ones2, p1, sm);
      f32x4 o = mfma16k16(vA, p0, f32x4{});
      o = mfma16k16(vp[t][hd], p1, o);
      const float inv = __builtin_amdgcn_rcpf(sm[0]);
      return v2u{pack2(o[0] * inv, o[1] * inv), pack2(o[2] * inv, o[3] * inv)};
    };
    {
      const bool ok = T0 + 32 * w > 0;
      const v2u o10 = attend(1, 0, kp[0][0], vp[0][0], true), o11 = attend(1, 1, kp[0][1], vp[0][1], true);
      const v2u o00 = attend(0, 0, kprev[0], vprev[0], ok), o01 = attend(0, 1, kprev[1], vprev[1], ok);
      stsc1(rsW, O_OFF + (u32)(((g * 8 + 2 * w + 1) * 16 + m) * 1024 + lane * 16), v4u{o10.x, o10.y, o11.x, o11.y});
      stsc1(rsW, O_OFF + (u32)(((g * 8 + 2 * w) * 16 + m) * 1024 + lane * 16), v4u{o00.x, o00.y, o01.x, o01.y});
    }
  }
  STAMP(2);
  WAITVN(0);
  publish(fl, m, tag);
  STAMP(3);

  {
    bf16x8 w2[8][4];
    int a[4];
    {
      const int row = 4 * q + qp, f = ((row >> 1) & 3) << 1;
#pragma unroll
      for (int n = 0; n < 4; ++n) a[n] = opq(W2_OFF + row * 128 + (((2 * n + (pp >> 1)) ^ f) * 16) + (pp & 1) * 8);
#pragma unroll
      for (int s = 0; s < 8; ++s)
#pragma unroll
        for (int n = 0; n < 4; ++n) w2[s][n] = tr2(a[n] + s * 4096, 2048);
    }
    poll(fl, m, 16, tag);
    BAR();
    STAMP(4);
    v4u of[16];
    const u32 ob = O_OFF + (u32)((g * 8 + 4 * th + w) * 16) * 1024;
    sfor<0, 16>([&](auto i) { ldsc1<0>(of[i.v], rsW, lane * 16, ob + (u32)(i.v & 15) * 1024); });
    f32x4 acc[4] = {};
    sfor<0, 16>([&](auto sc) {
      constexpr int s = sc.v;
      wait_for<15 - s>(of[s]);
      const bf16x8 bfr = __builtin_bit_cast(bf16x8, of[s]);
#pragma unroll
      for (int n = 0; n < 4; ++n) {
        acc[n] = mfma16(w2[s & 7][n], bfr, acc[n]);
        if constexpr (s < 8) w2[s][n] = tr2(a[n] + (s + 8) * 4096, 2048);
      }
      SCHED();
    });
    STAMP(5);
    float s = 0.f, ss = 0.f;
    const int tl = 16 * w + tok;
    const int t32 = g * 4 + 2 * th + (w >> 1);
    const u32 hoff = H_OFF + (u32)((t32 * 32 + 4 * ct) * 1024 + (16 * (w & 1) + tok + 32 * (q >> 1)) * 16 + (q & 1) * 8);
#pragma unroll
    for (int n = 0; n < 4; ++n) {
      const u32 h01 = pack2(bflo(xres[n].x) + acc[n][0], bfhi(xres[n].x) + acc[n][1]);
      const u32 h23 = pack2(bflo(xres[n].y) + acc[n][2], bfhi(xres[n].y) + acc[n][3]);
      const v2u hp = {h01, h23};
      stsc1_8(rsW, hoff + n * 1024, hp);
      *lp<v2u>(M_HK + tl * HK_PITCH + (16 * n + 4 * q) * 2) = hp;
      const float a0 = bflo(h01), a1 = bfhi(h01), a2 = bflo(h23), a3 = bfhi(h23);
      s += (a0 + a1) + (a2 + a3);
      ss = __builtin_fmaf(a0, a0, ss); ss = __builtin_fmaf(a1, a1, ss); ss = __builtin_fmaf(a2, a2, ss); ss = __builtin_fmaf(a3, a3, ss);
    }
    s = xadd32(xadd16(s)); ss = xadd32(xadd16(ss));
    if (q == 0) stsc1_8(rsW, ST_OFF + (u32)((ct * S + T0 + 64 * th + tl) * 8), v2u{asu(s), asu(ss)});
  }
  WAITVN(0);
  WAITL0();
  publish(fl + 256, m, tag);
  STAMP(6);

  {
    const int wa = (8 * hh + qp) * 192 + (q & 1) * 32 + pp * 8;
    bf16x8 wA[6], wB[6];
    auto w3_read1 = [&](bf16x8& dst, int base, int i) { const int o = base + (i & 1) * 12288 + (i >> 1) * 64; dst = tr2(o, 768); };
    poll(fl + 256, m, 16, tag);
    BAR();
    STAMP(7);
    const int t32 = g * 4 + w;
    v2u st[4];
#pragma unroll
    for (int i = 0; i < 4; ++i) ldsc1_8(st[i], rsW, (u32)(((hh * 4 + i) * S + l32) * 8), ST_OFF + (u32)(t32 * 32) * 8);
    v4u hf[16];
    const u32 hb = H_OFF + (u32)(t32 * 32) * 1024;
    auto ldh = [&](auto i) { ldsc1<(i.v & 3) * 1024>(hf[i.v & 15], rsW, lane * 16, hb + (u32)((i.v >> 2) & 7) * 4096); };
    sfor<0, 16>(ldh);
#pragma unroll
    for (int i = 0; i < 6; ++i) w3_read1(wA[i], wa, i);
    asm volatile("s_waitcnt vmcnt(16)" : "+v"(st[0]), "+v"(st[1]), "+v"(st[2]), "+v"(st[3])::"memory");
    float rs, nb;
    {
      float s = (asf(st[0].x) + asf(st[1].x)) + (asf(st[2].x) + asf(st[3].x)), ss = (asf(st[0].y) + asf(st[1].y)) + (asf(st[2].y) + asf(st[3].y));
      s = xadd32(s); ss = xadd32(ss);
      const float mu = s * (1.0f / D);
      rs = __builtin_amdgcn_rsqf(fmaxf(__builtin_fmaf(-mu, mu, ss * (1.0f / D)), 0.f) + 1e-5f);
      nb = -mu * rs;
    }
    auto ln = [&](v4u h, int d) {
      const f32x2 v = {bflo(h[d]), bfhi(h[d])};
      const f32x2 r = __builtin_elementwise_fma(v, f32x2{rs, rs}, f32x2{nb, nb});
      return pack2(r[0], r[1]);
    };
    v4u yA, yB;
    wait_for<15>(hf[0]);
#pragma unroll
    for (int d = 0; d < 4; ++d) yA[d] = ln(hf[0], d);
    ldh(IC<16>{});
    f32x16 acc[6] = {};
    auto step = [&](auto sc, bf16x8(&wc)[6], bf16x8(&wn)[6], v4u& yc, v4u& yn) {
      constexpr int s = sc.v, c1 = (s + 1) >> 2;
      if constexpr (s + 1 < 32) wait_for<(16 + s < 31 ? 16 + s : 31) - (s + 1)>(hf[(s + 1) & 15]);
      if constexpr (s >= 4 && (s & 3) == 0) BAR();
      const int base = opq(wa + (c1 % 6) * SLOT + (16 * ((s + 1) & 3)) * 192);
      const bf16x8 yb = __builtin_bit_cast(bf16x8, yc);
      __builtin_amdgcn_s_waitcnt(0xC07F);
      sfor<0, 6>([&](auto ic) {
        constexpr int i = ic.v;
        acc[i] = mfma32(wc[i], yb, acc[i]);
        if constexpr (s + 1 < 32) {
          if constexpr (i < 3) { w3_read1(wn[2 * i], base, 2 * i); w3_read1(wn[2 * i + 1], base, 2 * i + 1); }
          if constexpr (i >= 2) yn[i - 2] = ln(hf[(s + 1) & 15], i - 2);
        }
        if constexpr (i == 5 && 17 + s < 32) ldh(IC<(17 + s) & 31>{});
        SCHED();
      });
    };
    sfor<0, 14>([&](auto k) { step(IC<2 * k.v>{}, wA, wB, yA, yB); step(IC<2 * k.v + 1>{}, wB, wA, yB, yA); });
    {
      BAR();
      v4u y4[4];
      y4[0] = yA;
      sfor<1, 4>([&](auto j) {
        wait_for<3 - j.v>(hf[(28 + j.v) & 15]);
#pragma unroll
        for (int d = 0; d < 4; ++d) y4[j.v][d] = ln(hf[(28 + j.v) & 15], d);
      });
      const int base = opq(wa + (7 % 6) * SLOT);
      auto epi = [&](int i, int s) {
        v4u o;
#pragma unroll
        for (int d = 0; d < 4; ++d) {
          const int r = 8 * s + 2 * d;
          o[d] = pack2(fmaxf(acc[2 * i][r], 0.f) * acc[2 * i + 1][r], fmaxf(acc[2 * i][r + 1], 0.f) * acc[2 * i + 1][r + 1]);
        }
        const int f0 = 96 * m + 32 * i;
        if (f0 < F) stsc1(rsW, A_OFF + (u32)((t32 * 86 + f0 / 16 + s) * 1024 + lane * 16), o);
      };
      bf16x8 wl[3][8];
      auto rd_pair = [&](int i) {
#pragma unroll
        for (int j = 0; j < 4; ++j) { w3_read1(wl[i][2 * j], base + 16 * j * 192, 2 * i); w3_read1(wl[i][2 * j + 1], base + 16 * j * 192, 2 * i + 1); }
      };
      rd_pair(0); rd_pair(1);
      SCHED();
      sfor<0, 3>([&](auto ic) {
        constexpr int i = ic.v;
        if constexpr (i == 0) rd_pair(2);
        sfor<0, 4>([&](auto jc) {
          constexpr int j = jc.v;
          const bf16x8 yb = __builtin_bit_cast(bf16x8, y4[j]);
          acc[2 * i] = mfma32(wl[i][2 * j], yb, acc[2 * i]);
          acc[2 * i + 1] = mfma32(wl[i][2 * j + 1], yb, acc[2 * i + 1]);
          if constexpr (i > 0 && j == 1) epi(i - 1, 0);
          if constexpr (i > 0 && j == 3) epi(i - 1, 1);
          SCHED();
        });
      });
      STAMP(8);
      BAR();
      epi(2, 0); epi(2, 1);
    }
    WAITVN(0);
  }
  publish(fl + 512, m, tag);
  STAMP(9);

  {
    poll(fl + 512, m, 15, tag);
    BAR();
    STAMP(10);
    const int mh = w & 1, kh = w >> 1;
    const int t32 = g * 4 + 2 * th + mh;
    v4u af[36];
    const u32 ab = A_OFF + (u32)(t32 * 86 + kh * 43) * 1024;
    auto lda = [&](auto i) { const int ps = i.v; ldsc1<0>(af[i.v % 36], rsW, lane * 16, ab + (u32)(ps >= 43 ? ps - 43 : ps) * 1024); };
    sfor<0, 14>(lda);
    f32x16 acc[2] = {};
    int wa[2];
#pragma unroll
    for (int n = 0; n < 2; ++n) wa[n] = (4 * hh + qp) * 128 + ((n ^ ((qp >> 1) & 1)) * 64) + (q & 1) * 32 + pp * 8;
    bf16x8 wA[2], wB[2];
    auto w4_read = [&](bf16x8(&dst)[2], auto sc) {
      constexpr int s = sc.v;
      const int off = s < 42 ? ((s / 6 + 2) % 6) * SLOT + kh * 12288 + (s % 6) * 2048 : M_W4T + kh * 2048;
#pragma unroll
      for (int n = 0; n < 2; ++n) dst[n] = tr2(opq(wa[n] + off), 1024);
    };
    w4_read(wA, IC<0>{});
    auto step = [&](auto sc, bf16x8(&wc)[2], bf16x8(&wn)[2]) {
      constexpr int s = sc.v;
      constexpr int issued = (14 + 2 * (s < 8 ? s : 8) + (s > 8 ? s - 8 : 0)) < 43 ? (14 + 2 * (s < 8 ? s : 8) + (s > 8 ? s - 8 : 0)) : 43;
      wait_for<issued - 1 - s>(af[s % 36]);
      if constexpr (s == 6 || s == 30) BAR();
      if constexpr (s + 1 < 43) w4_read(wn, IC<s + 1>{});
      const bf16x8 bfr = __builtin_bit_cast(bf16x8, af[s % 36]);
      acc[0] = mfma32(wc[0], bfr, acc[0]);
      if constexpr (issued < 43) lda(IC<(issued < 43 ? issued : 0)>{});
      SCHED();
      acc[1] = mfma32(wc[1], bfr, acc[1]);
      if constexpr (s < 8 && issued + 1 < 43) lda(IC<(issued + 1 < 43 ? issued + 1 : 0)>{});
      SCHED();
    };
    sfor<0, 21>([&](auto k) { step(IC<2 * k.v>{}, wA, wB); step(IC<2 * k.v + 1>{}, wB, wA); });
    step(IC<42>{}, wA, wB);
    STAMP(11);
    LAS f32x4* red = lp<f32x4>(3 * SLOT);
    {
      const f32x16 snd = kh ? acc[0] : acc[1];
#pragma unroll
      for (int gg = 0; gg < 4; ++gg) red[(w * 4 + gg) * 64 + lane] = f32x4{snd[4 * gg], snd[4 * gg + 1], snd[4 * gg + 2], snd[4 * gg + 3]};
    }
    if (m == 0 && tid == 0) __hip_atomic_store(ep, tag, RLX_AGENT);
    WAITL0(); BAR();
    {
      const f32x16 keep = kh ? acc[1] : acc[0];
      const int pw = w ^ 2;
      const rsrc_t rsO = mkrs(p.out, S * D * 2);
      v2u r[4];
#pragma unroll
      for (int gg = 0; gg < 4; ++gg) {
        const f32x4 o = red[(pw * 4 + gg) * 64 + lane];
        const v2u hk = *lp<v2u>(M_HK + (32 * mh + l32) * HK_PITCH + (32 * kh + 8 * gg + 4 * hh) * 2);
        r[gg].x = pack2(bflo(hk.x) + (keep[4 * gg] + o[0]), bfhi(hk.x) + (keep[4 * gg + 1] + o[1]));
        r[gg].y = pack2(bflo(hk.y) + (keep[4 * gg + 2] + o[2]), bfhi(hk.y) + (keep[4 * gg + 3] + o[3]));
      }
      const u32 ob = (u32)(T0 + 64 * th + 32 * mh + l32) * (D * 2) + (u32)(64 * ct + 32 * kh + 8 * hh) * 2;
#pragma unroll
      for (int gp = 0; gp < 2; ++gp) {
        const auto sx = __builtin_amdgcn_permlane32_swap(r[2 * gp].x, r[2 * gp + 1].x, false, false);
        const auto sy = __builtin_amdgcn_permlane32_swap(r[2 * gp].y, r[2 * gp + 1].y, false, false);
        __builtin_amdgcn_raw_buffer_store_b128(v4u{sx[0], sy[0], sx[1], sy[1]}, rsO, ob + gp * 32, 0, 0);
      }
    }
  }
  STAMP(12);
}

extern "C" void kernel_launch(void* const* d_in, const int* in_sizes, int n_in, void* d_out, int out_size,
                              void* d_ws, size_t ws_size, hipStream_t stream) {
  Params p;
  p.x = (const u16*)d_in[0]; p.wqkv = (const u16*)d_in[1]; p.wo = (const u16*)d_in[2];
  p.wg = (const u16*)d_in[4]; p.wu = (const u16*)d_in[5]; p.wd = (const u16*)d_in[6];
  p.out = (u16*)d_out; p.ws = (unsigned char*)d_ws;
  (void)hipFuncSetAttribute((const void*)fused_block, hipFuncAttributeMaxDynamicSharedMemorySize, LDS_BYTES);
  fused_block<<<256, 512, LDS_BYTES, stream>>>(p);
}
```

```cpp
#include <hip/hip_runtime.h>
#include <stdint.h>

typedef unsigned short u16;
typedef unsigned u32;
typedef unsigned long long u64;
typedef short bf16x8 __attribute__((ext_vector_type(8)));
typedef short bf16x4 __attribute__((ext_vector_type(4)));
typedef float f32x2 __attribute__((ext_vector_type(2)));
typedef float f32x4 __attribute__((ext_vector_type(4)));
typedef float f32x16 __attribute__((ext_vector_type(16)));
typedef unsigned v4u __attribute__((ext_vector_type(4)));
typedef unsigned v2u __attribute__((ext_vector_type(2)));
typedef __bf16 bf2_t __attribute__((ext_vector_type(2)));
#define GAS __attribute__((address_space(1)))
#define LAS __attribute__((address_space(3)))
#define DI __device__ __forceinline__
#define RLX_AGENT __ATOMIC_RELAXED, __HIP_MEMORY_SCOPE_AGENT
typedef GAS u64 gu64;
typedef __amdgpu_buffer_rsrc_t rsrc_t;

constexpr int S = 2048, D = 512, F = 1376;
constexpr u32 O_OFF = 0;
constexpr u32 H_OFF = 2u << 20;
constexpr u32 A_OFF = 4u << 20;
constexpr u32 ST_OFF = 10u << 20;
constexpr u32 ST1_OFF = (10u << 20) + (512u << 10);
constexpr u32 FL_OFF = 11u << 20;
constexpr u32 EP_OFF = 12u << 20;
constexpr u32 WS_BYTES = 13u << 20;

constexpr int SLOT = 24576;
constexpr int MISC = 6 * SLOT;
constexpr int LDS_BYTES = 163840;
constexpr int P1_STAGE = 30720;
constexpr int P1_WOFF = 18432;
constexpr int W2_OFF = 3 * P1_STAGE;
constexpr int M_CS = 157696;
constexpr int M_MR = M_CS + 1024;
constexpr int KVH_OFF = M_CS + 2048;
constexpr int KV_OFF = 3 * SLOT;
constexpr int M_HK = MISC;
constexpr int M_W4T = MISC + 8704;
constexpr int HK_PITCH = 136;

struct Params {
  const u16* x; const u16* wqkv; const u16* wo; const u16* wg; const u16* wu; const u16* wd;
  u16* out; unsigned char* ws;
};

template <int I> struct IC { static constexpr int v = I; };
template <int I, int N, class Fn> DI void sfor(Fn&& f) { if constexpr (I < N) { f(IC<I>{}); sfor<I + 1, N>(f); } }

DI u32 pack2(float a, float b) { f32x2 v = {a, b}; bf2_t r = __builtin_convertvector(v, bf2_t); return __builtin_bit_cast(u32, r); }
DI float bflo(u32 u) { return __builtin_bit_cast(float, u << 16); }
DI float bfhi(u32 u) { return __builtin_bit_cast(float, u & 0xffff0000u); }
DI float asf(u32 u) { return __builtin_bit_cast(float, u); }
DI u32 asu(float f) { return __builtin_bit_cast(u32, f); }
DI f32x4 mfma16(bf16x8 a, bf16x8 b, f32x4 c) { return __builtin_amdgcn_mfma_f32_16x16x32_bf16(a, b, c, 0, 0, 0); }
DI f32x4 mfma16k16(v2u a, v2u b, f32x4 c) { return __builtin_amdgcn_mfma_f32_16x16x16bf16_1k(__builtin_bit_cast(bf16x4, a), __builtin_bit_cast(bf16x4, b), c, 0, 0, 0); }
DI f32x16 mfma32(bf16x8 a, bf16x8 b, f32x16 c) { return __builtin_amdgcn_mfma_f32_32x32x16_bf16(a, b, c, 0, 0, 0); }
DI bf16x8 cat(bf16x4 a, bf16x4 b) { return __builtin_shufflevector(a, b, 0, 1, 2, 3, 4, 5, 6, 7); }
#pragma clang diagnostic ignored "-Wint-to-pointer-cast"
DI bf16x4 trr(int off) { return __builtin_amdgcn_ds_read_tr16_b64_v4i16((LAS bf16x4*)(u32)off); }
DI bf16x8 tr2(int off, int d) { return cat(trr(off), trr(off + d)); }
DI bf16x8 ldsb(int off) { return *(LAS bf16x8*)(u32)off; }
template <class T> DI LAS T* lp(int off) { return (LAS T*)(u32)off; }
DI rsrc_t mkrs(const void* p, u32 bytes) { return __builtin_amdgcn_make_buffer_rsrc((void*)p, 0, bytes, 0x00020000); }
DI u32 rfl(u32 v) { return __builtin_amdgcn_readfirstlane(v); }
DI int opq(int v) { asm volatile("" : "+v"(v)); return v; }
DI float xmax16(float v) { auto r = __builtin_amdgcn_permlane16_swap(asu(v), asu(v), false, false); return fmaxf(asf(r[0]), asf(r[1])); }
DI float xmax32(float v) { auto r = __builtin_amdgcn_permlane32_swap(asu(v), asu(v), false, false); return fmaxf(asf(r[0]), asf(r[1])); }
DI float xadd16(float v) { auto r = __builtin_amdgcn_permlane16_swap(asu(v), asu(v), false, false); return asf(r[0]) + asf(r[1]); }
DI float xadd32(float v) { auto r = __builtin_amdgcn_permlane32_swap(asu(v), asu(v), false, false); return asf(r[0]) + asf(r[1]); }
#define BAR() __builtin_amdgcn_s_barrier()
#define SCHED() __builtin_amdgcn_sched_barrier(0)
#define WAITVN(n) asm volatile("s_waitcnt vmcnt(%0)" ::"i"(n) : "memory")
#define WAITL0() asm volatile("s_waitcnt lgkmcnt(0)" ::: "memory")

DI void dma(rsrc_t rs, u32 voff, u32 soff, u32 ldsaddr) {
  asm volatile("s_mov_b32 m0, %0\n\ts_nop 0\n\tbuffer_load_dwordx4 %1, %2, %3 offen lds" ::"s"(ldsaddr), "v"(voff), "s"(rs), "s"(soff) : "memory");
}
template <int IMM> DI void ldsc1(v4u& dst, rsrc_t rs, u32 voff, u32 soff) {
  asm volatile("buffer_load_dwordx4 %0, %1, %2, %3 offen offset:%4 sc1" : "=v"(dst) : "v"(voff), "s"(rs), "s"(soff), "i"(IMM) : "memory");
}
DI void ldsc1_8(v2u& dst, rsrc_t rs, u32 voff, u32 soff) {
  asm volatile("buffer_load_dwordx2 %0, %1, %2, %3 offen sc1" : "=v"(dst) : "v"(voff), "s"(rs), "s"(soff) : "memory");
}
DI void ld16(v4u& dst, rsrc_t rs, u32 voff, u32 soff) {
  asm volatile("buffer_load_dwordx4 %0, %1, %2, %3 offen" : "=v"(dst) : "v"(voff), "s"(rs), "s"(soff) : "memory");
}
DI void ld4_keep(u32& dst, rsrc_t rs, u32 voff) { asm volatile("buffer_load_dword %0, %1, %2, 0 offen" : "+v"(dst) : "v"(voff), "s"(rs) : "memory"); }
template <int IMM> DI void ld8(v2u& dst, rsrc_t rs, u32 voff, u32 soff) {
  asm volatile("buffer_load_dwordx2 %0, %1, %2, %3 offen offset:%4" : "=v"(dst) : "v"(voff), "s"(rs), "s"(soff), "i"(IMM) : "memory");
}
template <int N> DI void wait_for(v4u& a) { asm volatile("s_waitcnt vmcnt(%1)" : "+v"(a) : "i"(N > 63 ? 63 : N) : "memory"); }
DI void stsc1(rsrc_t rs, u32 off, v4u v) { __builtin_amdgcn_raw_buffer_store_b128(v, rs, off, 0, 16); }
DI void stsc1_8(rsrc_t rs, u32 off, v2u v) { __builtin_amdgcn_raw_buffer_store_b64(v, rs, off, 0, 16); }

#define STAMP(i) do {} while (0)

DI void publish(gu64* fl, int m, u64 tag) {
  BAR();
  if (threadIdx.x < 16) __hip_atomic_store(fl + threadIdx.x * 16 + m, tag, RLX_AGENT);
}
DI void poll(gu64* fl, int m, int nprod, u64 tag) {
  if (threadIdx.x < 64) {
    const int lane = threadIdx.x;
    for (u32 spins = 0; spins < (1u << 20); ++spins) {
      u64 v = tag;
      if (lane < nprod) v = __hip_atomic_load(fl + m * 16 + lane, RLX_AGENT);
      if (__all(v == tag)) break;
    }
    __builtin_amdgcn_fence(__ATOMIC_ACQUIRE, "wavefront");
  }
}

__global__ __launch_bounds__(512, 2) void fused_block(Params p) {
  extern __shared__ __attribute__((aligned(16))) unsigned char smem[];
  const int tid = threadIdx.x, lane = tid & 63, w8 = (int)rfl(tid >> 6), w = w8 & 3;
  const int b = blockIdx.x, g = ((b & 7) >> 1) * 4 + ((b >> 3) >> 3), m = (b & 1) * 8 + ((b >> 3) & 7);
  const int T0 = g * 128;
  const int tok = lane & 15, q = lane >> 4;
  const int th = m >> 3, ct = m & 7;
  const rsrc_t rsW = mkrs(p.ws, WS_BYTES);
  const rsrc_t rsX = mkrs(p.x, S * D * 2);
  const u32 halo0 = (u32)(T0 >= 16 ? T0 - 16 : 0);
  asm volatile("" ::"s"((u32)(size_t)smem));

  if (w8 >= 4) {
    const rsrc_t rsW1 = mkrs(p.wqkv, D * 3 * D * 2);
    v2u epv;
    ldsc1_8(epv, rsW, 0, EP_OFF + g * 128);
    u32 vx0, vx1, vxh, vw0, vw1, vw2;
    {
      const int r8 = lane >> 3, sl = lane & 7;
      vx0 = r8 * 1024 + ((sl ^ ((lane >> 4) & 7)) * 16);
      vx1 = r8 * 1024 + ((sl ^ ((4 + (lane >> 4)) & 7)) * 16);
      vxh = (w & 1) ? vx1 : vx0;
      auto pat = [&](int j) { const int e = j * 64 + lane, r = e / 12, pc = e % 12; return (u32)(r * 3072 + (pc >> 2) * 1024 + (pc & 3) * 16); };
      vw0 = pat(0); vw1 = pat(1); vw2 = pat(2);
    }
    const u32 sxh = (halo0 + 8 * (w & 1)) * 1024, sxm = (u32)(T0 + 32 * w) * 1024, sw1 = (u32)(16 * w) * 3072 + m * 64;
    const int rot = (g + m) & 7;
    auto p1_piece = [&](int cc, int j) {
      const u32 lb = (cc % 5) * P1_STAGE;
      const u32 c = (u32)((cc + rot) & 7);
      if (j == 0) dma(rsX, vxh, sxh + c * 128, lb + (w & 1) * 1024);
      if (j == 1) dma(rsW1, vw0, sw1 + c * (64 * 3072), lb + P1_WOFF + (3 * w) * 1024);
      if (j == 2) dma(rsX, vx0, sxm + c * 128, lb + (2 + 4 * w) * 1024);
      if (j == 3) dma(rsW1, vw1, sw1 + c * (64 * 3072), lb + P1_WOFF + (3 * w + 1) * 1024);
      if (j == 4) dma(rsX, vx1, sxm + c * 128 + 8192, lb + (3 + 4 * w) * 1024);
      if (j == 5) dma(rsW1, vw2, sw1 + c * (64 * 3072), lb + P1_WOFF + (3 * w + 2) * 1024);
      if (j == 6) dma(rsX, vx0, sxm + c * 128 + 16384, lb + (4 + 4 * w) * 1024);
      if (j == 7) dma(rsX, vx1, sxm + c * 128 + 24576, lb + (5 + 4 * w) * 1024);
    };
    const rsrc_t rsW2 = mkrs(p.wo, D * D * 2), rsG = mkrs(p.wg, D * F * 2), rsU = mkrs(p.wu, D * F * 2), rsD = mkrs(p.wd, F * D * 2);
    const u32 v2 = (lane >> 3) * 1024 + (((lane & 7) ^ (((lane >> 4) & 3) << 1)) * 16);
    auto w2_piece = [&](int i) {
      const int ii = 16 * w + i, ls = ii >> 2;
      dma(rsW2, v2, (u32)(32 * ((ls + 2 * ct) & 15) + 8 * (ii & 3)) * 1024 + ct * 128, W2_OFF + ii * 1024);
    };
    const int qp = (lane & 15) >> 2, pp = lane & 3;
    f32x4 ahA = {}, ahB = {};
    const int xha0 = tok * 128 + ((q ^ (tok >> 1)) * 16), xha1 = tok * 128 + (((4 + q) ^ (tok >> 1)) * 16);
    const int wha = P1_WOFF + (8 * q + qp) * 192 + (1 + (w >> 1)) * 64 + (w & 1) * 32 + pp * 8;
    const int whb = P1_WOFF + (8 * q + qp) * 192 + (w < 2 ? 0 : 128) + (w & 1) * 32 + pp * 8;
    auto halo_with = [&](int hc, auto&& issue) {
      const int sb = (hc % 5) * P1_STAGE;
      v4u xh[2]; bf16x8 fa[2], fb[2];
#pragma unroll
      for (int s2 = 0; s2 < 2; ++s2) {
        xh[s2] = __builtin_bit_cast(v4u, ldsb((s2 ? xha1 : xha0) + sb));
        fa[s2] = tr2(wha + sb + s2 * 6144, 768); fb[s2] = tr2(whb + sb + s2 * 6144, 768);
      }
      SCHED();
      issue(0); issue(1); issue(2); issue(3);
      SCHED();
#pragma unroll
      for (int s2 = 0; s2 < 2; ++s2) {
#pragma unroll
        for (int d = 0; d < 4; ++d) xh[s2][d] = tok == 0 ? 0x3F803F80u : xh[s2][d];
        const bf16x8 xhb = __builtin_bit_cast(bf16x8, xh[s2]);
        ahA = mfma16(fa[s2], xhb, ahA);
        ahB = mfma16(fb[s2], xhb, ahB);
      }
      SCHED();
      issue(4); issue(5); issue(6); issue(7);
    };
    sfor<0, 24>([&](auto i) { p1_piece(i.v >> 3, i.v & 7); });
    WAITVN(16); BAR();
    halo_with(0, [&](int j) { p1_piece(3, j); });
    WAITVN(16); BAR();
    halo_with(1, [&](int j) { p1_piece(4, j); });
    WAITVN(16); BAR();
    halo_with(2, [&](int j) { p1_piece(5, j); });
    WAITVN(16); BAR();
    halo_with(3, [&](int j) { p1_piece(6, j); });
    WAITVN(16); BAR();
    halo_with(4, [&](int j) { p1_piece(7, j); });
    u32 touch = 0;
    {
      const u32 wg_ = (u32)(b * 4 + w);
      const u32 o4 = (u32)lane < 4u ? (wg_ * 4u + lane) * 128u : 0xFFFFFF00u, o11 = (u32)lane < 11u ? (wg_ * 11u + lane) * 128u : 0xFFFFFF00u;
      ld4_keep(touch, rsW2, o4); ld4_keep(touch, rsG, o11); ld4_keep(touch, rsU, o11); ld4_keep(touch, rsD, o11);
    }
    v4u sth;
    ldsc1<0>(sth, rsW, ST1_OFF + (u32)(g * 4096 + tok * 16), 0);
    WAITVN(21); BAR();
    halo_with(5, [&](int) {});
    WAITVN(13); BAR();
    halo_with(6, [&](int) {});
    WAITVN(5); BAR();
    halo_with(7, [&](int j) { w2_piece(j); });
    asm volatile("s_waitcnt vmcnt(8)" : "+v"(sth), "+v"(touch), "+v"(epv)::"memory");
    const u64 tag = (((u64)rfl(epv.y) << 32) | rfl(epv.x)) + 1;
    const u32 tag32 = (u32)tag ^ (u32)(tag >> 32) ^ 0x9E3779B9u;
    for (u32 spins = 0; spins < (1u << 20); ++spins) {
      if (__all(sth.y == tag32 && sth.w == tag32)) break;
      __builtin_amdgcn_s_sleep(1);
      ldsc1<0>(sth, rsW, ST1_OFF + (u32)(g * 4096 + tok * 16), 0);
      wait_for<0>(sth);
    }
    {
      LAS float* cs_ = lp<float>(M_CS);
      if (tok == 0) {
        *(LAS f32x4*)(cs_ + 32 + 16 * w + 4 * q) = ahA;
        if (w < 2) *(LAS f32x4*)(cs_ + 16 * w + 4 * q) = ahB;
      }
      const float hmu = asf(sth.x), hrs = asf(sth.z);
      float hv[4];
#pragma unroll
      for (int r = 0; r < 4; ++r) {
        const float c = asf(__builtin_amdgcn_update_dpp(0, asu(ahA[r]), 0x150, 0xf, 0xf, false));
        hv[r] = hrs * __builtin_fmaf(-hmu, c, ahA[r]);
      }
      const u32 o0 = pack2(hv[0], hv[1]), o1 = pack2(hv[2], hv[3]);
      if (w < 2) *lp<v2u>(KVH_OFF + w * 512 + (tok + 16 * (((q & 1) << 1) | (q >> 1))) * 8) = v2u{o0, o1};
      else {
        LAS u16* vv = lp<u16>(KVH_OFF + w * 512) + (4 * q + 16 * (tok >> 2)) * 4 + (tok & 3);
        vv[0] = (u16)o0; vv[4] = (u16)(o0 >> 16); vv[8] = (u16)o1; vv[12] = (u16)(o1 >> 16);
      }
    }
    WAITL0();
    const u32 v30 = (lane / 12) * (F * 2) + (lane % 12) * 16, v31 = ((64 + lane) / 12) * (F * 2) + ((64 + lane) % 12) * 16,
              v32 = ((128 + lane) / 12) * (F * 2) + ((128 + lane) % 12) * 16;
    const u32 s3 = (u32)(16 * w) * (F * 2) + m * 192;
    auto w3_chunk = [&](int c) {
      const u32 so = s3 + (u32)(64 * ((c + m) & 7)) * (F * 2), lb = (c % 6) * SLOT + 3 * w * 1024;
      dma(rsG, v30, so, lb); dma(rsG, v31, so, lb + 1024); dma(rsG, v32, so, lb + 2048);
      dma(rsU, v30, so, lb + 12288); dma(rsU, v31, so, lb + 12288 + 1024); dma(rsU, v32, so, lb + 12288 + 2048);
    };
    const u32 v4s = (lane >> 3) * 1024 + (((lane & 7) ^ (((lane >> 4) & 1) << 2)) * 16);
    auto w4_row = [&](int ls) { const int ps = ls + 5 * ct; return (u32)((ps >= 43 ? ps - 43 : ps) * 16); };
    auto w4_part = [&](int part) {
      const u32 lb = ((part + 2) % 6) * SLOT + 3 * w * 1024;
#pragma unroll
      for (int j = 0; j < 3; ++j) {
        const int jj = 3 * w + j;
        const u32 so = (w4_row(6 * part + (jj >> 1)) + (jj & 1) * 8) * 1024 + ct * 128;
        dma(rsD, v4s, so, lb + j * 1024); dma(rsD, v4s, so + 688 * 1024, lb + 12288 + j * 1024);
      }
    };
    BAR();
    sfor<8, 16>([&](auto i) { w2_piece(i.v); });
    BAR();
    w3_chunk(0); w3_chunk(1);
    WAITVN(12);
    BAR();
    w3_chunk(2);
    BAR();
    BAR();
    w3_chunk(3);
    WAITVN(6); BAR();
    w3_chunk(4); w3_chunk(5);
    BAR(); w3_chunk(6);
    WAITVN(18); BAR(); w3_chunk(7);
    WAITVN(18); BAR(); w4_part(0);
    WAITVN(18); BAR(); w4_part(1);
    WAITVN(18); BAR(); w4_part(2);
    WAITVN(18); BAR(); w4_part(3);
    BAR(); w4_part(4);
    BAR();
    w4_part(5);
    dma(rsD, v4s, ((u32)(w >> 1) * 688 + w4_row(42) + (w & 1) * 8) * 1024 + ct * 128, M_W4T + w * 1024);
    BAR();
    WAITVN(7); BAR();
    WAITVN(0); BAR(); w4_part(6);
    WAITVN(0); BAR();
    BAR();
    return;
  }

  __builtin_amdgcn_s_setprio(2);
  STAMP(0);
  gu64* ep = (gu64*)(p.ws + EP_OFF) + g * 16;
  gu64* fl = (gu64*)(p.ws + FL_OFF) + g * 768;
  v2u epv, xres[4];
  v4u srow[3];
  ldsc1_8(epv, rsW, 0, EP_OFF + g * 128);
  sfor<0, 3>([&](auto jc) {
    const int j = jc.v < 2 ? w + 4 * jc.v : 8, R = 9 * m + j;
    const u32 token = R < 16 ? halo0 + R : (u32)(T0 + R - 16);
    ld16(srow[jc.v], rsX, lane * 16, token * 1024);
  });
  {
    const u32 vo = (u32)tok * 1024 + q * 8, so = (u32)(T0 + 64 * th + 16 * w) * 1024 + ct * 128;
    ld8<0>(xres[0], rsX, vo, so); ld8<32>(xres[1], rsX, vo, so); ld8<64>(xres[2], rsX, vo, so); ld8<96>(xres[3], rsX, vo, so);
  }
  u32 pre = 0;
  {
    const u32 t32a = (u32)(g * 4 + w);
    const u32 o = lane == 0 ? O_OFF + (u32)(g * 8 * 16) * 1024 : lane == 1 ? H_OFF + (u32)(g * 4 * 32) * 1024 : lane == 2 ? A_OFF + t32a * 86 * 1024 : lane == 3 ? ST_OFF + (u32)T0 * 8
                : lane == 4 ? FL_OFF + (u32)(g * 768) * 8 : 0xFFFFFF00u;
    ld4_keep(pre, rsW, o);
    ld4_keep(pre, mkrs(p.out, S * D * 2), lane == 0 ? (u32)T0 * (D * 2) : 0xFFFFFF00u);
  }
  asm volatile("s_waitcnt vmcnt(6)" : "+v"(epv), "+v"(srow[0]), "+v"(srow[1]), "+v"(srow[2])::"memory");
  const u64 tag = (((u64)rfl(epv.y) << 32) | rfl(epv.x)) + 1;
  const u32 tag32 = (u32)tag ^ (u32)(tag >> 32) ^ 0x9E3779B9u;
    {
    float sv[3], qv[3];
#pragma unroll
    for (int j = 0; j < 3; ++j) {
      float s = 0.f, ss = 0.f;
#pragma unroll
      for (int d = 0; d < 4; ++d) {
        const float a = bflo(srow[j][d]), c = bfhi(srow[j][d]);
        s += a + c; ss = __builtin_fmaf(a, a, ss); ss = __builtin_fmaf(c, c, ss);
      }
      sv[j] = s; qv[j] = ss;
    }
    auto s32 = [&](float a, float c) { auto r = __builtin_amdgcn_permlane32_swap(asu(a), asu(c), false, false); return asf(r[0]) + asf(r[1]); };
    auto s16 = [&](float a, float c) { auto r = __builtin_amdgcn_permlane16_swap(asu(a), asu(c), false, false); return asf(r[0]) + asf(r[1]); };
    const float A0 = s32(sv[0], qv[0]), A1 = s32(sv[1], qv[1]), A2 = s32(sv[2], qv[2]);
    float B = s16(A0, A1), C = s16(A2, A2);
    auto ror = [&](float v, auto n) { return v + asf(__builtin_amdgcn_update_dpp(0, asu(v), 0x120 + n.v, 0xf, 0xf, false)); };
    B = ror(B, IC<8>{}); C = ror(C, IC<8>{}); B = ror(B, IC<4>{}); C = ror(C, IC<4>{}); B = ror(B, IC<2>{}); C = ror(C, IC<2>{}); B = ror(B, IC<1>{}); C = ror(C, IC<1>{});
    const auto rb = __builtin_amdgcn_permlane32_swap(asu(B), asu(B), false, false), rc = __builtin_amdgcn_permlane32_swap(asu(C), asu(C), false, false);
    const float s = lane < 32 ? asf(rb[0]) : asf(rc[0]), ss = lane < 32 ? asf(rb[1]) : asf(rc[1]);
    const float mu = s * (1.0f / D), rs = __builtin_amdgcn_rsqf(fmaxf(__builtin_fmaf(-mu, mu, ss * (1.0f / D)), 0.f) + 1e-5f);
    const int j = q < 2 ? w + 4 * q : 8;
    if (tok == 0 && q < 3) stsc1(rsW, ST1_OFF + (u32)(g * 4096 + (9 * m + j) * 16), v4u{asu(mu), tag32, asu(rs), tag32});
  }

  const int l32 = lane & 31, hh = lane >> 5;
  const int qp = (lane & 15) >> 2, pp = lane & 3;
  {
    f32x16 aq = {}, ak = {}, av = {};
    int xa[4], wa1;
#pragma unroll
    for (int s4 = 0; s4 < 4; ++s4) xa[s4] = (16 + 32 * w + l32) * 128 + (((2 * s4 + hh) ^ ((l32 >> 1) & 7)) * 16);
    wa1 = P1_WOFF + (8 * hh + qp) * 192 + (q & 1) * 32 + pp * 8;
    constexpr float SC = 0.25f * 1.4426950408889634f, NEG = -3.0e38f;
    float bias0[4], bias1[4];
#pragma unroll
    for (int r = 0; r < 4; ++r) {
      const int d0 = tok + 16 - (4 * q + r), d1 = tok - (4 * q + r);
      bias0[r] = asf(opq(((d0 & 1) == 0 && d0 <= 14) ? 0 : (int)asu(NEG)));
      bias1[r] = asf(opq(((d1 & 1) == 0 && d1 >= 0 && d1 <= 14) ? 0 : (int)asu(NEG)));
    }
    struct P1F { bf16x8 xf; bf16x4 w[3][2]; };
    auto f_rd = [&](P1F& f, int u, int i) {
      const int sb = ((u >> 2) % 5) * P1_STAGE, s4 = u & 3;
      if (i == 0) f.xf = ldsb(xa[s4] + sb);
      else f.w[(i - 1) >> 1][(i - 1) & 1] = trr(wa1 + sb + s4 * 3072 + ((i - 1) >> 1) * 64 + ((i - 1) & 1) * 768);
    };
    P1F fa, fb;
    v4u st1;
    auto p1_step = [&](auto uc, P1F& f, P1F& n) {
      constexpr int u = uc.v;
      if constexpr ((u & 3) == 3 && u < 28) { WAITL0(); BAR(); }
      if constexpr (u == 24) ldsc1<0>(st1, rsW, ST1_OFF + (u32)(g * 4096 + (16 + 32 * w + l32) * 16), 0);
      sfor<0, 3>([&](auto ic) {
        constexpr int i = ic.v;
        const bf16x8 wf = cat(f.w[i][0], f.w[i][1]);
        if constexpr (i == 0) aq = mfma32(wf, f.xf, aq);
        if constexpr (i == 1) ak = mfma32(wf, f.xf, ak);
        if constexpr (i == 2) av = mfma32(f.xf, wf, av);
        if constexpr (u < 31) {
          if constexpr (i == 0) { f_rd(n, u + 1, 0); f_rd(n, u + 1, 1); f_rd(n, u + 1, 2); }
          if constexpr (i == 1) { f_rd(n, u + 1, 3); f_rd(n, u + 1, 4); }
          if constexpr (i == 2) { f_rd(n, u + 1, 5); f_rd(n, u + 1, 6); }
        }
        if constexpr (u == 29 && i == 2) {
          asm volatile("s_waitcnt vmcnt(0)" : "+v"(pre), "+v"(st1), "+v"(xres[0]), "+v"(xres[1]), "+v"(xres[2]), "+v"(xres[3])::"memory");
        }
        SCHED();
      });
    };
    BAR();
    sfor<0, 7>([&](auto i) { f_rd(fa, 0, i.v); });
    sfor<0, 15>([&](auto k) { p1_step(IC<2 * k.v>{}, fa, fb); p1_step(IC<2 * k.v + 1>{}, fb, fa); });
    for (u32 spins = 0; spins < (1u << 20); ++spins) {
      if (__all(st1.y == tag32 && st1.w == tag32)) break;
      __builtin_amdgcn_s_sleep(1);
      ldsc1<0>(st1, rsW, ST1_OFF + (u32)(g * 4096 + (16 + 32 * w + l32) * 16), 0);
      wait_for<0>(st1);
    }
    const float mean = asf(st1.x), rstd = asf(st1.z);
    LAS float* cs_ = lp<float>(M_CS);
    LAS float* mr = lp<float>(M_MR) + w * 64;
    if (lane < 32) { mr[lane] = mean; mr[32 + lane] = rstd; }
    p1_step(IC<30>{}, fa, fb);
    f32x4 mv[4], rv[4];
#pragma unroll
    for (int gg = 0; gg < 4; ++gg) { mv[gg] = *(LAS f32x4*)(mr + 8 * gg + 4 * hh); rv[gg] = *(LAS f32x4*)(mr + 32 + 8 * gg + 4 * hh); }
    p1_step(IC<31>{}, fb, fa);
    STAMP(1);
    BAR();
    f32x4 cq[4], ck[4];
#pragma unroll
    for (int gg = 0; gg < 4; ++gg) { cq[gg] = *(LAS f32x4*)(cs_ + 8 * gg + 4 * hh); ck[gg] = *(LAS f32x4*)(cs_ + 32 + 8 * gg + 4 * hh); }
    const float csv = cs_[64 + l32];
    SCHED();
    v2u qp_[2][2], kp[2][2], vp[2][2];
    {
      u32 pq[8], pk[8], pv[8];
#pragma unroll
      for (int gg = 0; gg < 4; ++gg) {
        float a[4], k4[4], v4[4];
#pragma unroll
        for (int r = 0; r < 4; ++r) {
          a[r] = __builtin_fmaf(-mean, cq[gg][r], aq[4 * gg + r]);
          k4[r] = rstd * __builtin_fmaf(-mean, ck[gg][r], ak[4 * gg + r]);
          v4[r] = rv[gg][r] * __builtin_fmaf(-mv[gg][r], csv, av[4 * gg + r]);
        }
        pq[2 * gg] = pack2(a[0], a[1]); pq[2 * gg + 1] = pack2(a[2], a[3]);
        pk[2 * gg] = pack2(k4[0], k4[1]); pk[2 * gg + 1] = pack2(k4[2], k4[3]);
        pv[2 * gg] = pack2(v4[0], v4[1]); pv[2 * gg + 1] = pack2(v4[2], v4[3]);
      }
#pragma unroll
      for (int x = 0; x < 2; ++x) {
#pragma unroll
        for (int d = 0; d < 2; ++d) {
          const auto sq_ = __builtin_amdgcn_permlane16_swap(pq[4 * x + d], pq[4 * x + 2 + d], false, false);
          qp_[0][x][d] = sq_[0]; qp_[1][x][d] = sq_[1];
          const auto sk_ = __builtin_amdgcn_permlane16_swap(pk[4 * x + d], pk[4 * x + 2 + d], false, false);
          kp[0][x][d] = sk_[0]; kp[1][x][d] = sk_[1];
          const auto s1 = __builtin_amdgcn_permlane32_swap(pv[4 * x + d], pv[4 * x + 2 + d], false, false);
          const auto s2 = __builtin_amdgcn_permlane16_swap(s1[0], s1[1], false, false);
          vp[x][0][d] = s2[0]; vp[x][1][d] = s2[1];
        }
      }
    }
    {
#pragma unroll
      for (int hd = 0; hd < 2; ++hd) {
        *lp<v2u>(KV_OFF + w * 2048 + hd * 512 + lane * 8) = kp[1][hd];
        *lp<v2u>(KV_OFF + w * 2048 + (2 + hd) * 512 + lane * 8) = vp[1][hd];
      }
    }
    WAITL0();
    BAR();
    v2u kprev[2], vprev[2];
    {
      const int pb = w == 0 ? KVH_OFF : KV_OFF + (w - 1) * 2048;
#pragma unroll
      for (int hd = 0; hd < 2; ++hd) {
        kprev[hd] = *lp<v2u>(pb + hd * 512 + lane * 8);
        vprev[hd] = *lp<v2u>(pb + (2 + hd) * 512 + lane * 8);
      }
    }
    const auto rq = __builtin_amdgcn_permlane16_swap(asu(rstd), asu(rstd), false, false);
    const v2u ones2 = {0x3F803F80u, 0x3F803F80u};
    auto attend = [&](int t, int hd, v2u kA, v2u vA, bool prev_ok) {
      const float sc = SC * asf(rq[t]);
      f32x4 s0 = mfma16k16(kA, qp_[t][hd], f32x4{});
      f32x4 s1 = mfma16k16(kp[t][hd], qp_[t][hd], f32x4{});
      float e[8];
#pragma unroll
      for (int r = 0; r < 4; ++r) {
        e[r] = __builtin_fmaf(s0[r], sc, prev_ok ? bias0[r] : NEG);
        e[4 + r] = __builtin_fmaf(s1[r], sc, bias1[r]);
      }
      float mx = fmaxf(fmaxf(fmaxf(e[0], e[1]), fmaxf(e[2], e[3])), fmaxf(fmaxf(e[4], e[5]), fmaxf(e[6], e[7])));
      mx = xmax32(xmax16(mx));
#pragma unroll
      for (int r = 0; r < 8; ++r) e[r] = __builtin_amdgcn_exp2f(e[r] - mx);
      const v2u p0 = {pack2(e[0], e[1]), pack2(e[2], e[3])}, p1 = {pack2(e[4], e[5]), pack2(e[6], e[7])};
      f32x4 sm = mfma16k16(ones2, p0, f32x4{});
      sm = mfma16k16(ones2, p1, sm);
      f32x4 o = mfma16k16(vA, p0, f32x4{});
      o = mfma16k16(vp[t][hd], p1, o);
      const float inv = __builtin_amdgcn_rcpf(sm[0]);
      return v2u{pack2(o[0] * inv, o[1] * inv), pack2(o[2] * inv, o[3] * inv)};
    };
    {
      const bool ok = T0 + 32 * w > 0;
      const v2u o10 = attend(1, 0, kp[0][0], vp[0][0], true), o11 = attend(1, 1, kp[0][1], vp[0][1], true);
      const v2u o00 = attend(0, 0, kprev[0], vprev[0], ok), o01 = attend(0, 1, kprev[1], vprev[1], ok);
      stsc1(rsW, O_OFF + (u32)(((g * 8 + 2 * w + 1) * 16 + m) * 1024 + lane * 16), v4u{o10.x, o10.y, o11.x, o11.y});
      stsc1(rsW, O_OFF + (u32)(((g * 8 + 2 * w) * 16 + m) * 1024 + lane * 16), v4u{o00.x, o00.y, o01.x, o01.y});
    }
  }
  STAMP(2);
  WAITVN(0);
  publish(fl, m, tag);
  STAMP(3);

  {
    bf16x8 w2[8][4];
    int a[4];
    {
      const int row = 4 * q + qp, f = ((row >> 1) & 3) << 1;
#pragma unroll
      for (int n = 0; n < 4; ++n) a[n] = opq(W2_OFF + row * 128 + (((2 * n + (pp >> 1)) ^ f) * 16) + (pp & 1) * 8);
#pragma unroll
      for (int s = 0; s < 8; ++s)
#pragma unroll
        for (int n = 0; n < 4; ++n) w2[s][n] = tr2(a[n] + s * 4096, 2048);
    }
    poll(fl, m, 16, tag);
    BAR();
    STAMP(4);
    v4u of[16];
    const u32 ob = O_OFF + (u32)((g * 8 + 4 * th + w) * 16) * 1024;
    sfor<0, 16>([&](auto i) { ldsc1<0>(of[i.v], rsW, lane * 16, ob + (u32)((i.v + 2 * ct) & 15) * 1024); });
    f32x4 acc[4] = {};
    sfor<0, 16>([&](auto sc) {
      constexpr int s = sc.v;
      wait_for<15 - s>(of[s]);
      const bf16x8 bfr = __builtin_bit_cast(bf16x8, of[s]);
#pragma unroll
      for (int n = 0; n < 4; ++n) {
        acc[n] = mfma16(w2[s & 7][n], bfr, acc[n]);
        if constexpr (s < 8) w2[s][n] = tr2(a[n] + (s + 8) * 4096, 2048);
      }
      SCHED();
    });
    STAMP(5);
    float s = 0.f, ss = 0.f;
    const int tl = 16 * w + tok;
    const int t32 = g * 4 + 2 * th + (w >> 1);
    const u32 hoff = H_OFF + (u32)((t32 * 32 + 4 * ct) * 1024 + (16 * (w & 1) + tok + 32 * (q >> 1)) * 16 + (q & 1) * 8);
#pragma unroll
    for (int n = 0; n < 4; ++n) {
      const u32 h01 = pack2(bflo(xres[n].x) + acc[n][0], bfhi(xres[n].x) + acc[n][1]);
      const u32 h23 = pack2(bflo(xres[n].y) + acc[n][2], bfhi(xres[n].y) + acc[n][3]);
      const v2u hp = {h01, h23};
      stsc1_8(rsW, hoff + n * 1024, hp);
      *lp<v2u>(M_HK + tl * HK_PITCH + (16 * n + 4 * q) * 2) = hp;
      const float a0 = bflo(h01), a1 = bfhi(h01), a2 = bflo(h23), a3 = bfhi(h23);
      s += (a0 + a1) + (a2 + a3);
      ss = __builtin_fmaf(a0, a0, ss); ss = __builtin_fmaf(a1, a1, ss); ss = __builtin_fmaf(a2, a2, ss); ss = __builtin_fmaf(a3, a3, ss);
    }
    s = xadd32(xadd16(s)); ss = xadd32(xadd16(ss));
    if (q == 0) stsc1_8(rsW, ST_OFF + (u32)((ct * S + T0 + 64 * th + tl) * 8), v2u{asu(s), asu(ss)});
  }
  WAITVN(0);
  WAITL0();
  publish(fl + 256, m, tag);
  STAMP(6);

  {
    const int wa = (8 * hh + qp) * 192 + (q & 1) * 32 + pp * 8;
    bf16x8 wA[6], wB[6];
    auto w3_read1 = [&](bf16x8& dst, int base, int i) { const int o = base + (i & 1) * 12288 + (i >> 1) * 64; dst = tr2(o, 768); };
    poll(fl + 256, m, 16, tag);
    BAR();
    STAMP(7);
    const int t32 = g * 4 + w;
    v2u st[4];
#pragma unroll
    for (int i = 0; i < 4; ++i) ldsc1_8(st[i], rsW, (u32)(((hh * 4 + i) * S + l32) * 8), ST_OFF + (u32)(t32 * 32) * 8);
    v4u hf[16];
    const u32 hb = H_OFF + (u32)(t32 * 32) * 1024;
    auto ldh = [&](auto i) { ldsc1<(i.v & 3) * 1024>(hf[i.v & 15], rsW, lane * 16, hb + (u32)(((i.v >> 2) + m) & 7) * 4096); };
    sfor<0, 16>(ldh);
#pragma unroll
    for (int i = 0; i < 6; ++i) w3_read1(wA[i], wa, i);
    asm volatile("s_waitcnt vmcnt(16)" : "+v"(st[0]), "+v"(st[1]), "+v"(st[2]), "+v"(st[3])::"memory");
    float rs, nb;
    {
      float s = (asf(st[0].x) + asf(st[1].x)) + (asf(st[2].x) + asf(st[3].x)), ss = (asf(st[0].y) + asf(st[1].y)) + (asf(st[2].y) + asf(st[3].y));
      s = xadd32(s); ss = xadd32(ss);
      const float mu = s * (1.0f / D);
      rs = __builtin_amdgcn_rsqf(fmaxf(__builtin_fmaf(-mu, mu, ss * (1.0f / D)), 0.f) + 1e-5f);
      nb = -mu * rs;
    }
    auto ln = [&](v4u h, int d) {
      const f32x2 v = {bflo(h[d]), bfhi(h[d])};
      const f32x2 r = __builtin_elementwise_fma(v, f32x2{rs, rs}, f32x2{nb, nb});
      return pack2(r[0], r[1]);
    };
    v4u yA, yB;
    wait_for<15>(hf[0]);
#pragma unroll
    for (int d = 0; d < 4; ++d) yA[d] = ln(hf[0], d);
    ldh(IC<16>{});
    f32x16 acc[6] = {};
    auto step = [&](auto sc, bf16x8(&wc)[6], bf16x8(&wn)[6], v4u& yc, v4u& yn) {
      constexpr int s = sc.v, c1 = (s + 1) >> 2;
      if constexpr (s + 1 < 32) wait_for<(16 + s < 31 ? 16 + s : 31) - (s + 1)>(hf[(s + 1) & 15]);
      if constexpr (s >= 4 && (s & 3) == 0) BAR();
      const int base = opq(wa + (c1 % 6) * SLOT + (16 * ((s + 1) & 3)) * 192);
      const bf16x8 yb = __builtin_bit_cast(bf16x8, yc);
      __builtin_amdgcn_s_waitcnt(0xC07F);
      sfor<0, 6>([&](auto ic) {
        constexpr int i = ic.v;
        acc[i] = mfma32(wc[i], yb, acc[i]);
        if constexpr (s + 1 < 32) {
          if constexpr (i < 3) { w3_read1(wn[2 * i], base, 2 * i); w3_read1(wn[2 * i + 1], base, 2 * i + 1); }
          if constexpr (i >= 2) yn[i - 2] = ln(hf[(s + 1) & 15], i - 2);
        }
        if constexpr (i == 5 && 17 + s < 32) ldh(IC<(17 + s) & 31>{});
        SCHED();
      });
    };
    sfor<0, 14>([&](auto k) { step(IC<2 * k.v>{}, wA, wB, yA, yB); step(IC<2 * k.v + 1>{}, wB, wA, yB, yA); });
    {
      BAR();
      v4u y4[4];
      y4[0] = yA;
      sfor<1, 4>([&](auto j) {
        wait_for<3 - j.v>(hf[(28 + j.v) & 15]);
#pragma unroll
        for (int d = 0; d < 4; ++d) y4[j.v][d] = ln(hf[(28 + j.v) & 15], d);
      });
      const int base = opq(wa + (7 % 6) * SLOT);
      auto epi = [&](int i, int s) {
        v4u o;
#pragma unroll
        for (int d = 0; d < 4; ++d) {
          const int r = 8 * s + 2 * d;
          o[d] = pack2(fmaxf(acc[2 * i][r], 0.f) * acc[2 * i + 1][r], fmaxf(acc[2 * i][r + 1], 0.f) * acc[2 * i + 1][r + 1]);
        }
        const int f0 = 96 * m + 32 * i;
        if (f0 < F) stsc1(rsW, A_OFF + (u32)((t32 * 86 + f0 / 16 + s) * 1024 + lane * 16), o);
      };
      bf16x8 wl[3][8];
      auto rd_pair = [&](int i) {
#pragma unroll
        for (int j = 0; j < 4; ++j) { w3_read1(wl[i][2 * j], base + 16 * j * 192, 2 * i); w3_read1(wl[i][2 * j + 1], base + 16 * j * 192, 2 * i + 1); }
      };
      rd_pair(0); rd_pair(1);
      SCHED();
      sfor<0, 3>([&](auto ic) {
        constexpr int i = ic.v;
        if constexpr (i == 0) rd_pair(2);
        sfor<0, 4>([&](auto jc) {
          constexpr int j = jc.v;
          const bf16x8 yb = __builtin_bit_cast(bf16x8, y4[j]);
          acc[2 * i] = mfma32(wl[i][2 * j], yb, acc[2 * i]);
          acc[2 * i + 1] = mfma32(wl[i][2 * j + 1], yb, acc[2 * i + 1]);
          if constexpr (i > 0 && j == 1) epi(i - 1, 0);
          if constexpr (i > 0 && j == 3) epi(i - 1, 1);
          SCHED();
        });
      });
      STAMP(8);
      BAR();
      epi(2, 0); epi(2, 1);
    }
    WAITVN(0);
  }
  publish(fl + 512, m, tag);
  STAMP(9);

  {
    poll(fl + 512, m, 15, tag);
    BAR();
    STAMP(10);
    const int mh = w & 1, kh = w >> 1;
    const int t32 = g * 4 + 2 * th + mh;
    v4u af[36];
    const u32 ab = A_OFF + (u32)(t32 * 86 + kh * 43) * 1024;
    auto lda = [&](auto i) { const int ps = i.v + 5 * ct; ldsc1<0>(af[i.v % 36], rsW, lane * 16, ab + (u32)(ps >= 43 ? ps - 43 : ps) * 1024); };
    sfor<0, 14>(lda);
    f32x16 acc[2] = {};
    int wa[2];
#pragma unroll
    for (int n = 0; n < 2; ++n) wa[n] = (4 * hh + qp) * 128 + ((n ^ ((qp >> 1) & 1)) * 64) + (q & 1) * 32 + pp * 8;
    bf16x8 wA[2], wB[2];
    auto w4_read = [&](bf16x8(&dst)[2], auto sc) {
      constexpr int s = sc.v;
      const int off = s < 42 ? ((s / 6 + 2) % 6) * SLOT + kh * 12288 + (s % 6) * 2048 : M_W4T + kh * 2048;
#pragma unroll
      for (int n = 0; n < 2; ++n) dst[n] = tr2(opq(wa[n] + off), 1024);
    };
    w4_read(wA, IC<0>{});
    auto step = [&](auto sc, bf16x8(&wc)[2], bf16x8(&wn)[2]) {
      constexpr int s = sc.v;
      constexpr int issued = (14 + 2 * (s < 8 ? s : 8) + (s > 8 ? s - 8 : 0)) < 43 ? (14 + 2 * (s < 8 ? s : 8) + (s > 8 ? s - 8 : 0)) : 43;
      wait_for<issued - 1 - s>(af[s % 36]);
      if constexpr (s == 6 || s == 30) BAR();
      if constexpr (s + 1 < 43) w4_read(wn, IC<s + 1>{});
      const bf16x8 bfr = __builtin_bit_cast(bf16x8, af[s % 36]);
      acc[0] = mfma32(wc[0], bfr, acc[0]);
      if constexpr (issued < 43) lda(IC<(issued < 43 ? issued : 0)>{});
      SCHED();
      acc[1] = mfma32(wc[1], bfr, acc[1]);
      if constexpr (s < 8 && issued + 1 < 43) lda(IC<(issued + 1 < 43 ? issued + 1 : 0)>{});
      SCHED();
    };
    sfor<0, 21>([&](auto k) { step(IC<2 * k.v>{}, wA, wB); step(IC<2 * k.v + 1>{}, wB, wA); });
    step(IC<42>{}, wA, wB);
    STAMP(11);
    LAS f32x4* red = lp<f32x4>(3 * SLOT);
    {
      const f32x16 snd = kh ? acc[0] : acc[1];
#pragma unroll
      for (int gg = 0; gg < 4; ++gg) red[(w * 4 + gg) * 64 + lane] = f32x4{snd[4 * gg], snd[4 * gg + 1], snd[4 * gg + 2], snd[4 * gg + 3]};
    }
    if (m == 0 && tid == 0) __hip_atomic_store(ep, tag, RLX_AGENT);
    WAITL0(); BAR();
    {
      const f32x16 keep = kh ? acc[1] : acc[0];
      const int pw = w ^ 2;
      const rsrc_t rsO = mkrs(p.out, S * D * 2);
      v2u r[4];
#pragma unroll
      for (int gg = 0; gg < 4; ++gg) {
        const f32x4 o = red[(pw * 4 + gg) * 64 + lane];
        const v2u hk = *lp<v2u>(M_HK + (32 * mh + l32) * HK_PITCH + (32 * kh + 8 * gg + 4 * hh) * 2);
        r[gg].x = pack2(bflo(hk.x) + (keep[4 * gg] + o[0]), bfhi(hk.x) + (keep[4 * gg + 1] + o[1]));
        r[gg].y = pack2(bflo(hk.y) + (keep[4 * gg + 2] + o[2]), bfhi(hk.y) + (keep[4 * gg + 3] + o[3]));
      }
      const u32 ob = (u32)(T0 + 64 * th + 32 * mh + l32) * (D * 2) + (u32)(64 * ct + 32 * kh + 8 * hh) * 2;
#pragma unroll
      for (int gp = 0; gp < 2; ++gp) {
        const auto sx = __builtin_amdgcn_permlane32_swap(r[2 * gp].x, r[2 * gp + 1].x, false, false);
        const auto sy = __builtin_amdgcn_permlane32_swap(r[2 * gp].y, r[2 * gp + 1].y, false, false);
        __builtin_amdgcn_raw_buffer_store_b128(v4u{sx[0], sy[0], sx[1], sy[1]}, rsO, ob + gp * 32, 0, 0);
      }
    }
  }
  STAMP(12);
}

extern "C" void kernel_launch(void* const* d_in, const int* in_sizes, int n_in, void* d_out, int out_size,
                              void* d_ws, size_t ws_size, hipStream_t stream) {
  Params p;
  p.x = (const u16*)d_in[0]; p.wqkv = (const u16*)d_in[1]; p.wo = (const u16*)d_in[2];
  p.wg = (const u16*)d_in[4]; p.wu = (const u16*)d_in[5]; p.wd = (const u16*)d_in[6];
  p.out = (u16*)d_out; p.ws = (unsigned char*)d_ws;
  (void)hipFuncSetAttribute((const void*)fused_block, hipFuncAttributeMaxDynamicSharedMemorySize, LDS_BYTES);
  fused_block<<<256, 512, LDS_BYTES, stream>>>(p);
}
```

```cpp
#include <hip/hip_runtime.h>
#include <stdint.h>

typedef unsigned short u16;
typedef unsigned u32;
typedef unsigned long long u64;
typedef short bf16x8 __attribute__((ext_vector_type(8)));
typedef short bf16x4 __attribute__((ext_vector_type(4)));
typedef float f32x2 __attribute__((ext_vector_type(2)));
typedef float f32x4 __attribute__((ext_vector_type(4)));
typedef float f32x16 __attribute__((ext_vector_type(16)));
typedef unsigned v4u __attribute__((ext_vector_type(4)));
typedef unsigned v2u __attribute__((ext_vector_type(2)));
typedef __bf16 bf2_t __attribute__((ext_vector_type(2)));
#define GAS __attribute__((address_space(1)))
#define LAS __attribute__((address_space(3)))
#define DI __device__ __forceinline__
#define RLX_AGENT __ATOMIC_RELAXED, __HIP_MEMORY_SCOPE_AGENT
typedef GAS u64 gu64;
typedef __amdgpu_buffer_rsrc_t rsrc_t;

constexpr int S = 2048, D = 512, F = 1376;
constexpr u32 O_OFF = 0;
constexpr u32 H_OFF = 2u << 20;
constexpr u32 A_OFF = 4u << 20;
constexpr u32 ST_OFF = 10u << 20;
constexpr u32 ST1_OFF = (10u << 20) + (512u << 10);
constexpr u32 FL_OFF = 11u << 20;
constexpr u32 EP_OFF = 12u << 20;
constexpr u32 WS_BYTES = 13u << 20;

constexpr int SLOT = 24576;
constexpr int MISC = 6 * SLOT;
constexpr int LDS_BYTES = 163840;
constexpr int P1_STAGE = 30720;
constexpr int P1_WOFF = 18432;
constexpr int W2_OFF = 3 * P1_STAGE;
constexpr int M_CS = 157696;
constexpr int M_MR = M_CS + 1024;
constexpr int KVH_OFF = M_CS + 2048;
constexpr int KV_OFF = 3 * SLOT;
constexpr int M_HK = MISC;
constexpr int M_W4T = MISC + 8704;
constexpr int HK_PITCH = 136;

struct Params {
  const u16* x; const u16* wqkv; const u16* wo; const u16* wg; const u16* wu; const u16* wd;
  u16* out; unsigned char* ws;
};

template <int I> struct IC { static constexpr int v = I; };
template <int I, int N, class Fn> DI void sfor(Fn&& f) { if constexpr (I < N) { f(IC<I>{}); sfor<I + 1, N>(f); } }

DI u32 pack2(float a, float b) { f32x2 v = {a, b}; bf2_t r = __builtin_convertvector(v, bf2_t); return __builtin_bit_cast(u32, r); }
DI float bflo(u32 u) { return __builtin_bit_cast(float, u << 16); }
DI float bfhi(u32 u) { return __builtin_bit_cast(float, u & 0xffff0000u); }
DI float asf(u32 u) { return __builtin_bit_cast(float, u); }
DI u32 asu(float f) { return __builtin_bit_cast(u32, f); }
DI f32x4 mfma16(bf16x8 a, bf16x8 b, f32x4 c) { return __builtin_amdgcn_mfma_f32_16x16x32_bf16(a, b, c, 0, 0, 0); }
DI f32x4 mfma16k16(v2u a, v2u b, f32x4 c) { return __builtin_amdgcn_mfma_f32_16x16x16bf16_1k(__builtin_bit_cast(bf16x4, a), __builtin_bit_cast(bf16x4, b), c, 0, 0, 0); }
DI f32x16 mfma32(bf16x8 a, bf16x8 b, f32x16 c) { return __builtin_amdgcn_mfma_f32_32x32x16_bf16(a, b, c, 0, 0, 0); }
DI bf16x8 cat(bf16x4 a, bf16x4 b) { return __builtin_shufflevector(a, b, 0, 1, 2, 3, 4, 5, 6, 7); }
#pragma clang diagnostic ignored "-Wint-to-pointer-cast"
DI bf16x4 trr(int off) { return __builtin_amdgcn_ds_read_tr16_b64_v4i16((LAS bf16x4*)(u32)off); }
DI bf16x8 tr2(int off, int d) { return cat(trr(off), trr(off + d)); }
DI bf16x8 ldsb(int off) { return *(LAS bf16x8*)(u32)off; }
template <class T> DI LAS T* lp(int off) { return (LAS T*)(u32)off; }
DI rsrc_t mkrs(const void* p, u32 bytes) { return __builtin_amdgcn_make_buffer_rsrc((void*)p, 0, bytes, 0x00020000); }
DI u32 rfl(u32 v) { return __builtin_amdgcn_readfirstlane(v); }
DI int opq(int v) { asm volatile("" : "+v"(v)); return v; }
DI float xmax16(float v) { auto r = __builtin_amdgcn_permlane16_swap(asu(v), asu(v), false, false); return fmaxf(asf(r[0]), asf(r[1])); }
DI float xmax32(float v) { auto r = __builtin_amdgcn_permlane32_swap(asu(v), asu(v), false, false); return fmaxf(asf(r[0]), asf(r[1])); }
DI float xadd16(float v) { auto r = __builtin_amdgcn_permlane16_swap(asu(v), asu(v), false, false); return asf(r[0]) + asf(r[1]); }
DI float xadd32(float v) { auto r = __builtin_amdgcn_permlane32_swap(asu(v), asu(v), false, false); return asf(r[0]) + asf(r[1]); }
#define BAR() __builtin_amdgcn_s_barrier()
#define SCHED() __builtin_amdgcn_sched_barrier(0)
#define WAITVN(n) asm volatile("s_waitcnt vmcnt(%0)" ::"i"(n) : "memory")
#define WAITL0() asm volatile("s_waitcnt lgkmcnt(0)" ::: "memory")

DI void dma(rsrc_t rs, u32 voff, u32 soff, u32 ldsaddr) {
  asm volatile("s_mov_b32 m0, %0\n\ts_nop 0\n\tbuffer_load_dwordx4 %1, %2, %3 offen lds" ::"s"(ldsaddr), "v"(voff), "s"(rs), "s"(soff) : "memory");
}
template <int IMM> DI void ldsc1(v4u& dst, rsrc_t rs, u32 voff, u32 soff) {
  asm volatile("buffer_load_dwordx4 %0, %1, %2, %3 offen offset:%4 sc1" : "=v"(dst) : "v"(voff), "s"(rs), "s"(soff), "i"(IMM) : "memory");
}
DI void ldsc1_8(v2u& dst, rsrc_t rs, u32 voff, u32 soff) {
  asm volatile("buffer_load_dwordx2 %0, %1, %2, %3 offen sc1" : "=v"(dst) : "v"(voff), "s"(rs), "s"(soff) : "memory");
}
DI void ld16(v4u& dst, rsrc_t rs, u32 voff, u32 soff) {
  asm volatile("buffer_load_dwordx4 %0, %1, %2, %3 offen" : "=v"(dst) : "v"(voff), "s"(rs), "s"(soff) : "memory");
}
DI void ld4_keep(u32& dst, rsrc_t rs, u32 voff) { asm volatile("buffer_load_dword %0, %1, %2, 0 offen" : "+v"(dst) : "v"(voff), "s"(rs) : "memory"); }
template <int IMM> DI void ld8(v2u& dst, rsrc_t rs, u32 voff, u32 soff) {
  asm volatile("buffer_load_dwordx2 %0, %1, %2, %3 offen offset:%4" : "=v"(dst) : "v"(voff), "s"(rs), "s"(soff), "i"(IMM) : "memory");
}
template <int N> DI void wait_for(v4u& a) { asm volatile("s_waitcnt vmcnt(%1)" : "+v"(a) : "i"(N > 63 ? 63 : N) : "memory"); }
DI void stsc1(rsrc_t rs, u32 off, v4u v) { __builtin_amdgcn_raw_buffer_store_b128(v, rs, off, 0, 16); }
DI void stsc1_8(rsrc_t rs, u32 off, v2u v) { __builtin_amdgcn_raw_buffer_store_b64(v, rs, off, 0, 16); }


DI void publish(gu64* fl, int m, u64 tag) {
  BAR();
  if (threadIdx.x < 16) __hip_atomic_store(fl + threadIdx.x * 16 + m, tag, RLX_AGENT);
}
DI void poll(gu64* fl, int m, int nprod, u64 tag) {
  if (threadIdx.x < 64) {
    const int lane = threadIdx.x;
    for (u32 spins = 0; spins < (1u << 20); ++spins) {
      u64 v = tag;
      if (lane < nprod) v = __hip_atomic_load(fl + m * 16 + lane, RLX_AGENT);
      if (__all(v == tag)) break;
    }
    __builtin_amdgcn_fence(__ATOMIC_ACQUIRE, "wavefront");
  }
}

__global__ __launch_bounds__(512, 2) void fused_block(Params p) {
  extern __shared__ __attribute__((aligned(16))) unsigned char smem[];
  const int tid = threadIdx.x, lane = tid & 63, w8 = (int)rfl(tid >> 6), w = w8 & 3;
  const int b = blockIdx.x, g = ((b & 7) >> 1) * 4 + ((b >> 3) >> 3), m = (b & 1) * 8 + ((b >> 3) & 7);
  const int T0 = g * 128;
  const int tok = lane & 15, q = lane >> 4;
  const int th = m >> 3, ct = m & 7;
  const rsrc_t rsW = mkrs(p.ws, WS_BYTES);
  const rsrc_t rsX = mkrs(p.x, S * D * 2);
  const u32 halo0 = (u32)(T0 >= 16 ? T0 - 16 : 0);
  asm volatile("" ::"s"((u32)(size_t)smem));

  if (w8 >= 4) {
    const rsrc_t rsW1 = mkrs(p.wqkv, D * 3 * D * 2);
    v2u epv;
    ldsc1_8(epv, rsW, 0, EP_OFF + g * 128);
    u32 vx0, vx1, vxh, vw0, vw1, vw2;
    {
      const int r8 = lane >> 3, sl = lane & 7;
      vx0 = r8 * 1024 + ((sl ^ ((lane >> 4) & 7)) * 16);
      vx1 = r8 * 1024 + ((sl ^ ((4 + (lane >> 4)) & 7)) * 16);
      vxh = (w & 1) ? vx1 : vx0;
      auto pat = [&](int j) { const int e = j * 64 + lane, r = e / 12, pc = e % 12; return (u32)(r * 3072 + (pc >> 2) * 1024 + (pc & 3) * 16); };
      vw0 = pat(0); vw1 = pat(1); vw2 = pat(2);
    }
    const u32 sxh = (halo0 + 8 * (w & 1)) * 1024, sxm = (u32)(T0 + 32 * w) * 1024, sw1 = (u32)(16 * w) * 3072 + m * 64;
    const int rot = (g + m) & 7;
    auto p1_piece = [&](int cc, int j) {
      const u32 lb = (cc % 5) * P1_STAGE;
      const u32 c = (u32)((cc + rot) & 7);
      if (j == 0) dma(rsX, vxh, sxh + c * 128, lb + (w & 1) * 1024);
      if (j == 1) dma(rsW1, vw0, sw1 + c * (64 * 3072), lb + P1_WOFF + (3 * w) * 1024);
      if (j == 2) dma(rsX, vx0, sxm + c * 128, lb + (2 + 4 * w) * 1024);
      if (j == 3) dma(rsW1, vw1, sw1 + c * (64 * 3072), lb + P1_WOFF + (3 * w + 1) * 1024);
      if (j == 4) dma(rsX, vx1, sxm + c * 128 + 8192, lb + (3 + 4 * w) * 1024);
      if (j == 5) dma(rsW1, vw2, sw1 + c * (64 * 3072), lb + P1_WOFF + (3 * w + 2) * 1024);
      if (j == 6) dma(rsX, vx0, sxm + c * 128 + 16384, lb + (4 + 4 * w) * 1024);
      if (j == 7) dma(rsX, vx1, sxm + c * 128 + 24576, lb + (5 + 4 * w) * 1024);
    };
    const rsrc_t rsW2 = mkrs(p.wo, D * D * 2), rsG = mkrs(p.wg, D * F * 2), rsU = mkrs(p.wu, D * F * 2), rsD = mkrs(p.wd, F * D * 2);
    const u32 v2 = (lane >> 3) * 1024 + (((lane & 7) ^ (((lane >> 4) & 3) << 1)) * 16);
    auto w2_piece = [&](int i) {
      const int ii = 16 * w + i, ls = ii >> 2;
      dma(rsW2, v2, (u32)(32 * ((ls + 2 * ct) & 15) + 8 * (ii & 3)) * 1024 + ct * 128, W2_OFF + ii * 1024);
    };
    const int qp = (lane & 15) >> 2, pp = lane & 3;
    f32x4 ahA = {}, ahB = {};
    const int xha0 = tok * 128 + ((q ^ (tok >> 1)) * 16), xha1 = tok * 128 + (((4 + q) ^ (tok >> 1)) * 16);
    const int wha = P1_WOFF + (8 * q + qp) * 192 + (1 + (w >> 1)) * 64 + (w & 1) * 32 + pp * 8;
    const int whb = P1_WOFF + (8 * q + qp) * 192 + (w < 2 ? 0 : 128) + (w & 1) * 32 + pp * 8;
    auto halo_with = [&](int hc, auto&& issue) {
      const int sb = (hc % 5) * P1_STAGE;
      v4u xh[2]; bf16x8 fa[2], fb[2];
#pragma unroll
      for (int s2 = 0; s2 < 2; ++s2) {
        xh[s2] = __builtin_bit_cast(v4u, ldsb((s2 ? xha1 : xha0) + sb));
        fa[s2] = tr2(wha + sb + s2 * 6144, 768); fb[s2] = tr2(whb + sb + s2 * 6144, 768);
      }
      SCHED();
      issue(0); issue(1); issue(2); issue(3);
      SCHED();
#pragma unroll
      for (int s2 = 0; s2 < 2; ++s2) {
#pragma unroll
        for (int d = 0; d < 4; ++d) xh[s2][d] = tok == 0 ? 0x3F803F80u : xh[s2][d];
        const bf16x8 xhb = __builtin_bit_cast(bf16x8, xh[s2]);
        ahA = mfma16(fa[s2], xhb, ahA);
        ahB = mfma16(fb[s2], xhb, ahB);
      }
      SCHED();
      issue(4); issue(5); issue(6); issue(7);
    };
    sfor<0, 24>([&](auto i) { p1_piece(i.v >> 3, i.v & 7); });
    WAITVN(16); BAR();
    halo_with(0, [&](int j) { p1_piece(3, j); });
    WAITVN(16); BAR();
    halo_with(1, [&](int j) { p1_piece(4, j); });
    WAITVN(16); BAR();
    halo_with(2, [&](int j) { p1_piece(5, j); });
    WAITVN(16); BAR();
    halo_with(3, [&](int j) { p1_piece(6, j); });
    WAITVN(16); BAR();
    halo_with(4, [&](int j) { p1_piece(7, j); });
    u32 touch = 0;
    {
      const u32 wg_ = (u32)(b * 4 + w);
      const u32 o4 = (u32)lane < 4u ? (wg_ * 4u + lane) * 128u : 0xFFFFFF00u, o11 = (u32)lane < 11u ? (wg_ * 11u + lane) * 128u : 0xFFFFFF00u;
      ld4_keep(touch, rsW2, o4); ld4_keep(touch, rsG, o11); ld4_keep(touch, rsU, o11); ld4_keep(touch, rsD, o11);
    }
    v4u sth;
    ldsc1<0>(sth, rsW, ST1_OFF + (u32)(g * 4096 + tok * 16), 0);
    WAITVN(21); BAR();
    halo_with(5, [&](int) {});
    WAITVN(13); BAR();
    halo_with(6, [&](int) {});
    WAITVN(5); BAR();
    halo_with(7, [&](int j) { w2_piece(j); });
    asm volatile("s_waitcnt vmcnt(8)" : "+v"(sth), "+v"(touch), "+v"(epv)::"memory");
    const u64 tag = (((u64)rfl(epv.y) << 32) | rfl(epv.x)) + 1;
    const u32 tag32 = (u32)tag ^ (u32)(tag >> 32) ^ 0x9E3779B9u;
    for (u32 spins = 0; spins < (1u << 20); ++spins) {
      if (__all(sth.y == tag32 && sth.w == tag32)) break;
      __builtin_amdgcn_s_sleep(1);
      ldsc1<0>(sth, rsW, ST1_OFF + (u32)(g * 4096 + tok * 16), 0);
      wait_for<0>(sth);
    }
    {
      LAS float* cs_ = lp<float>(M_CS);
      if (tok == 0) {
        *(LAS f32x4*)(cs_ + 32 + 16 * w + 4 * q) = ahA;
        if (w < 2) *(LAS f32x4*)(cs_ + 16 * w + 4 * q) = ahB;
      }
      const float hmu = asf(sth.x), hrs = asf(sth.z);
      float hv[4];
#pragma unroll
      for (int r = 0; r < 4; ++r) {
        const float c = asf(__builtin_amdgcn_update_dpp(0, asu(ahA[r]), 0x150, 0xf, 0xf, false));
        hv[r] = hrs * __builtin_fmaf(-hmu, c, ahA[r]);
      }
      const u32 o0 = pack2(hv[0], hv[1]), o1 = pack2(hv[2], hv[3]);
      if (w < 2) *lp<v2u>(KVH_OFF + w * 512 + (tok + 16 * (((q & 1) << 1) | (q >> 1))) * 8) = v2u{o0, o1};
      else {
        LAS u16* vv = lp<u16>(KVH_OFF + w * 512) + (4 * q + 16 * (tok >> 2)) * 4 + (tok & 3);
        vv[0] = (u16)o0; vv[4] = (u16)(o0 >> 16); vv[8] = (u16)o1; vv[12] = (u16)(o1 >> 16);
      }
    }
    WAITL0();
    const u32 v30 = (lane / 12) * (F * 2) + (lane % 12) * 16, v31 = ((64 + lane) / 12) * (F * 2) + ((64 + lane) % 12) * 16,
              v32 = ((128 + lane) / 12) * (F * 2) + ((128 + lane) % 12) * 16;
    const u32 s3 = (u32)(16 * w) * (F * 2) + m * 192;
    auto w3_chunk = [&](int c) {
      const u32 so = s3 + (u32)(64 * ((c + m) & 7)) * (F * 2), lb = (c % 6) * SLOT + 3 * w * 1024;
      dma(rsG, v30, so, lb); dma(rsG, v31, so, lb + 1024); dma(rsG, v32, so, lb + 2048);
      dma(rsU, v30, so, lb + 12288); dma(rsU, v31, so, lb + 12288 + 1024); dma(rsU, v32, so, lb + 12288 + 2048);
    };
    const u32 v4s = (lane >> 3) * 1024 + (((lane & 7) ^ (((lane >> 4) & 1) << 2)) * 16);
    auto w4_row = [&](int ls) { const int ps = ls + 5 * ct; return (u32)((ps >= 43 ? ps - 43 : ps) * 16); };
    auto w4_part = [&](int part) {
      const u32 lb = ((part + 2) % 6) * SLOT + 3 * w * 1024;
#pragma unroll
      for (int j = 0; j < 3; ++j) {
        const int jj = 3 * w + j;
        const u32 so = (w4_row(6 * part + (jj >> 1)) + (jj & 1) * 8) * 1024 + ct * 128;
        dma(rsD, v4s, so, lb + j * 1024); dma(rsD, v4s, so + 688 * 1024, lb + 12288 + j * 1024);
      }
    };
    BAR();
    sfor<8, 16>([&](auto i) { w2_piece(i.v); });
    BAR();
    w3_chunk(0); w3_chunk(1);
    WAITVN(12);
    BAR();
    w3_chunk(2);
    BAR();
    BAR();
    w3_chunk(3);
    WAITVN(6); BAR();
    w3_chunk(4); w3_chunk(5);
    BAR(); w3_chunk(6);
    WAITVN(18); BAR(); w3_chunk(7);
    WAITVN(18); BAR(); w4_part(0);
    WAITVN(18); BAR(); w4_part(1);
    WAITVN(18); BAR(); w4_part(2);
    WAITVN(18); BAR(); w4_part(3);
    BAR(); w4_part(4);
    BAR();
    w4_part(5);
    dma(rsD, v4s, ((u32)(w >> 1) * 688 + w4_row(42) + (w & 1) * 8) * 1024 + ct * 128, M_W4T + w * 1024);
    BAR();
    WAITVN(7); BAR();
    WAITVN(0); BAR(); w4_part(6);
    WAITVN(0); BAR();
    BAR();
    return;
  }

  __builtin_amdgcn_s_setprio(2);
  gu64* ep = (gu64*)(p.ws + EP_OFF) + g * 16;
  gu64* fl = (gu64*)(p.ws + FL_OFF) + g * 768;
  v2u epv, xres[4];
  v4u srow[3];
  ldsc1_8(epv, rsW, 0, EP_OFF + g * 128);
  sfor<0, 3>([&](auto jc) {
    const int j = jc.v < 2 ? w + 4 * jc.v : 8, R = 9 * m + j;
    const u32 token = R < 16 ? halo0 + R : (u32)(T0 + R - 16);
    ld16(srow[jc.v], rsX, lane * 16, token * 1024);
  });
  {
    const u32 vo = (u32)tok * 1024 + q * 8, so = (u32)(T0 + 64 * th + 16 * w) * 1024 + ct * 128;
    ld8<0>(xres[0], rsX, vo, so); ld8<32>(xres[1], rsX, vo, so); ld8<64>(xres[2], rsX, vo, so); ld8<96>(xres[3], rsX, vo, so);
  }
  u32 pre = 0;
  {
    const u32 t32a = (u32)(g * 4 + w);
    const u32 o = lane == 0 ? O_OFF + (u32)(g * 8 * 16) * 1024 : lane == 1 ? H_OFF + (u32)(g * 4 * 32) * 1024 : lane == 2 ? A_OFF + t32a * 86 * 1024 : lane == 3 ? ST_OFF + (u32)T0 * 8
                : lane == 4 ? FL_OFF + (u32)(g * 768) * 8 : 0xFFFFFF00u;
    ld4_keep(pre, rsW, o);
    ld4_keep(pre, mkrs(p.out, S * D * 2), lane == 0 ? (u32)T0 * (D * 2) : 0xFFFFFF00u);
  }
  asm volatile("s_waitcnt vmcnt(6)" : "+v"(epv), "+v"(srow[0]), "+v"(srow[1]), "+v"(srow[2])::"memory");
  const u64 tag = (((u64)rfl(epv.y) << 32) | rfl(epv.x)) + 1;
  const u32 tag32 = (u32)tag ^ (u32)(tag >> 32) ^ 0x9E3779B9u;
    {
    float sv[3], qv[3];
#pragma unroll
    for (int j = 0; j < 3; ++j) {
      float s = 0.f, ss = 0.f;
#pragma unroll
      for (int d = 0; d < 4; ++d) {
        const float a = bflo(srow[j][d]), c = bfhi(srow[j][d]);
        s += a + c; ss = __builtin_fmaf(a, a, ss); ss = __builtin_fmaf(c, c, ss);
      }
      sv[j] = s; qv[j] = ss;
    }
    auto s32 = [&](float a, float c) { auto r = __builtin_amdgcn_permlane32_swap(asu(a), asu(c), false, false); return asf(r[0]) + asf(r[1]); };
    auto s16 = [&](float a, float c) { auto r = __builtin_amdgcn_permlane16_swap(asu(a), asu(c), false, false); return asf(r[0]) + asf(r[1]); };
    const float A0 = s32(sv[0], qv[0]), A1 = s32(sv[1], qv[1]), A2 = s32(sv[2], qv[2]);
    float B = s16(A0, A1), C = s16(A2, A2);
    auto ror = [&](float v, auto n) { return v + asf(__builtin_amdgcn_update_dpp(0, asu(v), 0x120 + n.v, 0xf, 0xf, false)); };
    B = ror(B, IC<8>{}); C = ror(C, IC<8>{}); B = ror(B, IC<4>{}); C = ror(C, IC<4>{}); B = ror(B, IC<2>{}); C = ror(C, IC<2>{}); B = ror(B, IC<1>{}); C = ror(C, IC<1>{});
    const auto rb = __builtin_amdgcn_permlane32_swap(asu(B), asu(B), false, false), rc = __builtin_amdgcn_permlane32_swap(asu(C), asu(C), false, false);
    const float s = lane < 32 ? asf(rb[0]) : asf(rc[0]), ss = lane < 32 ? asf(rb[1]) : asf(rc[1]);
    const float mu = s * (1.0f / D), rs = __builtin_amdgcn_rsqf(fmaxf(__builtin_fmaf(-mu, mu, ss * (1.0f / D)), 0.f) + 1e-5f);
    const int j = q < 2 ? w + 4 * q : 8;
    if (tok == 0 && q < 3) stsc1(rsW, ST1_OFF + (u32)(g * 4096 + (9 * m + j) * 16), v4u{asu(mu), tag32, asu(rs), tag32});
  }

  const int l32 = lane & 31, hh = lane >> 5;
  const int qp = (lane & 15) >> 2, pp = lane & 3;
  {
    f32x16 aq = {}, ak = {}, av = {};
    int xa[4], wa1;
#pragma unroll
    for (int s4 = 0; s4 < 4; ++s4) xa[s4] = (16 + 32 * w + l32) * 128 + (((2 * s4 + hh) ^ ((l32 >> 1) & 7)) * 16);
    wa1 = P1_WOFF + (8 * hh + qp) * 192 + (q & 1) * 32 + pp * 8;
    constexpr float SC = 0.25f * 1.4426950408889634f, NEG = -3.0e38f;
    float bias0[4], bias1[4];
#pragma unroll
    for (int r = 0; r < 4; ++r) {
      const int d0 = tok + 16 - (4 * q + r), d1 = tok - (4 * q + r);
      bias0[r] = asf(opq(((d0 & 1) == 0 && d0 <= 14) ? 0 : (int)asu(NEG)));
      bias1[r] = asf(opq(((d1 & 1) == 0 && d1 >= 0 && d1 <= 14) ? 0 : (int)asu(NEG)));
    }
    struct P1F { bf16x8 xf; bf16x4 w[3][2]; };
    auto f_rd = [&](P1F& f, int u, int i) {
      const int sb = ((u >> 2) % 5) * P1_STAGE, s4 = u & 3;
      if (i == 0) f.xf = ldsb(xa[s4] + sb);
      else f.w[(i - 1) >> 1][(i - 1) & 1] = trr(wa1 + sb + s4 * 3072 + ((i - 1) >> 1) * 64 + ((i - 1) & 1) * 768);
    };
    P1F fa, fb;
    v4u st1;
    auto p1_step = [&](auto uc, P1F& f, P1F& n) {
      constexpr int u = uc.v;
      if constexpr ((u & 3) == 3 && u < 28) { WAITL0(); BAR(); }
      if constexpr (u == 24) ldsc1<0>(st1, rsW, ST1_OFF + (u32)(g * 4096 + (16 + 32 * w + l32) * 16), 0);
      sfor<0, 3>([&](auto ic) {
        constexpr int i = ic.v;
        const bf16x8 wf = cat(f.w[i][0], f.w[i][1]);
        if constexpr (i == 0) aq = mfma32(wf, f.xf, aq);
        if constexpr (i == 1) ak = mfma32(wf, f.xf, ak);
        if constexpr (i == 2) av = mfma32(f.xf, wf, av);
        if constexpr (u < 31) {
          if constexpr (i == 0) { f_rd(n, u + 1, 0); f_rd(n, u + 1, 1); f_rd(n, u + 1, 2); }
          if constexpr (i == 1) { f_rd(n, u + 1, 3); f_rd(n, u + 1, 4); }
          if constexpr (i == 2) { f_rd(n, u + 1, 5); f_rd(n, u + 1, 6); }
        }
        if constexpr (u == 29 && i == 2) {
          asm volatile("s_waitcnt vmcnt(0)" : "+v"(pre), "+v"(st1), "+v"(xres[0]), "+v"(xres[1]), "+v"(xres[2]), "+v"(xres[3])::"memory");
        }
        SCHED();
      });
    };
    BAR();
    sfor<0, 7>([&](auto i) { f_rd(fa, 0, i.v); });
    sfor<0, 15>([&](auto k) { p1_step(IC<2 * k.v>{}, fa, fb); p1_step(IC<2 * k.v + 1>{}, fb, fa); });
    for (u32 spins = 0; spins < (1u << 20); ++spins) {
      if (__all(st1.y == tag32 && st1.w == tag32)) break;
      __builtin_amdgcn_s_sleep(1);
      ldsc1<0>(st1, rsW, ST1_OFF + (u32)(g * 4096 + (16 + 32 * w + l32) * 16), 0);
      wait_for<0>(st1);
    }
    const float mean = asf(st1.x), rstd = asf(st1.z);
    LAS float* cs_ = lp<float>(M_CS);
    LAS float* mr = lp<float>(M_MR) + w * 64;
    if (lane < 32) { mr[lane] = mean; mr[32 + lane] = rstd; }
    p1_step(IC<30>{}, fa, fb);
    f32x4 mv[4], rv[4];
#pragma unroll
    for (int gg = 0; gg < 4; ++gg) { mv[gg] = *(LAS f32x4*)(mr + 8 * gg + 4 * hh); rv[gg] = *(LAS f32x4*)(mr + 32 + 8 * gg + 4 * hh); }
    p1_step(IC<31>{}, fb, fa);
    BAR();
    f32x4 cq[4], ck[4];
#pragma unroll
    for (int gg = 0; gg < 4; ++gg) { cq[gg] = *(LAS f32x4*)(cs_ + 8 * gg + 4 * hh); ck[gg] = *(LAS f32x4*)(cs_ + 32 + 8 * gg + 4 * hh); }
    const float csv = cs_[64 + l32];
    SCHED();
    v2u qp_[2][2], kp[2][2], vp[2][2];
    {
      u32 pq[8], pk[8], pv[8];
#pragma unroll
      for (int gg = 0; gg < 4; ++gg) {
        float a[4], k4[4], v4[4];
#pragma unroll
        for (int r = 0; r < 4; ++r) {
          a[r] = __builtin_fmaf(-mean, cq[gg][r], aq[4 * gg + r]);
          k4[r] = rstd * __builtin_fmaf(-mean, ck[gg][r], ak[4 * gg + r]);
          v4[r] = rv[gg][r] * __builtin_fmaf(-mv[gg][r], csv, av[4 * gg + r]);
        }
        pq[2 * gg] = pack2(a[0], a[1]); pq[2 * gg + 1] = pack2(a[2], a[3]);
        pk[2 * gg] = pack2(k4[0], k4[1]); pk[2 * gg + 1] = pack2(k4[2], k4[3]);
        pv[2 * gg] = pack2(v4[0], v4[1]); pv[2 * gg + 1] = pack2(v4[2], v4[3]);
      }
#pragma unroll
      for (int x = 0; x < 2; ++x) {
#pragma unroll
        for (int d = 0; d < 2; ++d) {
          const auto sq_ = __builtin_amdgcn_permlane16_swap(pq[4 * x + d], pq[4 * x + 2 + d], false, false);
          qp_[0][x][d] = sq_[0]; qp_[1][x][d] = sq_[1];
          const auto sk_ = __builtin_amdgcn_permlane16_swap(pk[4 * x + d], pk[4 * x + 2 + d], false, false);
          kp[0][x][d] = sk_[0]; kp[1][x][d] = sk_[1];
          const auto s1 = __builtin_amdgcn_permlane32_swap(pv[4 * x + d], pv[4 * x + 2 + d], false, false);
          const auto s2 = __builtin_amdgcn_permlane16_swap(s1[0], s1[1], false, false);
          vp[x][0][d] = s2[0]; vp[x][1][d] = s2[1];
        }
      }
    }
    {
#pragma unroll
      for (int hd = 0; hd < 2; ++hd) {
        *lp<v2u>(KV_OFF + w * 2048 + hd * 512 + lane * 8) = kp[1][hd];
        *lp<v2u>(KV_OFF + w * 2048 + (2 + hd) * 512 + lane * 8) = vp[1][hd];
      }
    }
    WAITL0();
    BAR();
    v2u kprev[2], vprev[2];
    {
      const int pb = w == 0 ? KVH_OFF : KV_OFF + (w - 1) * 2048;
#pragma unroll
      for (int hd = 0; hd < 2; ++hd) {
        kprev[hd] = *lp<v2u>(pb + hd * 512 + lane * 8);
        vprev[hd] = *lp<v2u>(pb + (2 + hd) * 512 + lane * 8);
      }
    }
    const auto rq = __builtin_amdgcn_permlane16_swap(asu(rstd), asu(rstd), false, false);
    const v2u ones2 = {0x3F803F80u, 0x3F803F80u};
    auto attend = [&](int t, int hd, v2u kA, v2u vA, bool prev_ok) {
      const float sc = SC * asf(rq[t]);
      f32x4 s0 = mfma16k16(kA, qp_[t][hd], f32x4{});
      f32x4 s1 = mfma16k16(kp[t][hd], qp_[t][hd], f32x4{});
      float e[8];
#pragma unroll
      for (int r = 0; r < 4; ++r) {
        e[r] = __builtin_fmaf(s0[r], sc, prev_ok ? bias0[r] : NEG);
        e[4 + r] = __builtin_fmaf(s1[r], sc, bias1[r]);
      }
      float mx = fmaxf(fmaxf(fmaxf(e[0], e[1]), fmaxf(e[2], e[3])), fmaxf(fmaxf(e[4], e[5]), fmaxf(e[6], e[7])));
      mx = xmax32(xmax16(mx));
#pragma unroll
      for (int r = 0; r < 8; ++r) e[r] = __builtin_amdgcn_exp2f(e[r] - mx);
      const v2u p0 = {pack2(e[0], e[1]), pack2(e[2], e[3])}, p1 = {pack2(e[4], e[5]), pack2(e[6], e[7])};
      f32x4 sm = mfma16k16(ones2, p0, f32x4{});
      sm = mfma16k16(ones2, p1, sm);
      f32x4 o = mfma16k16(vA, p0, f32x4{});
      o = mfma16k16(vp[t][hd], p1, o);
      const float inv = __builtin_amdgcn_rcpf(sm[0]);
      return v2u{pack2(o[0] * inv, o[1] * inv), pack2(o[2] * inv, o[3] * inv)};
    };
    {
      const bool ok = T0 + 32 * w > 0;
      const v2u o10 = attend(1, 0, kp[0][0], vp[0][0], true), o11 = attend(1, 1, kp[0][1], vp[0][1], true);
      const v2u o00 = attend(0, 0, kprev[0], vprev[0], ok), o01 = attend(0, 1, kprev[1], vprev[1], ok);
      stsc1(rsW, O_OFF + (u32)(((g * 8 + 2 * w + 1) * 16 + m) * 1024 + lane * 16), v4u{o10.x, o10.y, o11.x, o11.y});
      stsc1(rsW, O_OFF + (u32)(((g * 8 + 2 * w) * 16 + m) * 1024 + lane * 16), v4u{o00.x, o00.y, o01.x, o01.y});
    }
  }
  WAITVN(0);
  publish(fl, m, tag);

  {
    bf16x8 w2[8][4];
    int a[4];
    {
      const int row = 4 * q + qp, f = ((row >> 1) & 3) << 1;
#pragma unroll
      for (int n = 0; n < 4; ++n) a[n] = opq(W2_OFF + row * 128 + (((2 * n + (pp >> 1)) ^ f) * 16) + (pp & 1) * 8);
#pragma unroll
      for (int s = 0; s < 8; ++s)
#pragma unroll
        for (int n = 0; n < 4; ++n) w2[s][n] = tr2(a[n] + s * 4096, 2048);
    }
    poll(fl, m, 16, tag);
    BAR();
    v4u of[16];
    const u32 ob = O_OFF + (u32)((g * 8 + 4 * th + w) * 16) * 1024;
    sfor<0, 16>([&](auto i) { ldsc1<0>(of[i.v], rsW, lane * 16, ob + (u32)((i.v + 2 * ct) & 15) * 1024); });
    f32x4 acc[4] = {};
    sfor<0, 16>([&](auto sc) {
      constexpr int s = sc.v;
      wait_for<15 - s>(of[s]);
      const bf16x8 bfr = __builtin_bit_cast(bf16x8, of[s]);
#pragma unroll
      for (int n = 0; n < 4; ++n) {
        acc[n] = mfma16(w2[s & 7][n], bfr, acc[n]);
        if constexpr (s < 8) w2[s][n] = tr2(a[n] + (s + 8) * 4096, 2048);
      }
      SCHED();
    });
    float s = 0.f, ss = 0.f;
    const int tl = 16 * w + tok;
    const int t32 = g * 4 + 2 * th + (w >> 1);
    const u32 hoff = H_OFF + (u32)((t32 * 32 + 4 * ct) * 1024 + (16 * (w & 1) + tok + 32 * (q >> 1)) * 16 + (q & 1) * 8);
#pragma unroll
    for (int n = 0; n < 4; ++n) {
      const u32 h01 = pack2(bflo(xres[n].x) + acc[n][0], bfhi(xres[n].x) + acc[n][1]);
      const u32 h23 = pack2(bflo(xres[n].y) + acc[n][2], bfhi(xres[n].y) + acc[n][3]);
      const v2u hp = {h01, h23};
      stsc1_8(rsW, hoff + n * 1024, hp);
      *lp<v2u>(M_HK + tl * HK_PITCH + (16 * n + 4 * q) * 2) = hp;
      const float a0 = bflo(h01), a1 = bfhi(h01), a2 = bflo(h23), a3 = bfhi(h23);
      s += (a0 + a1) + (a2 + a3);
      ss = __builtin_fmaf(a0, a0, ss); ss = __builtin_fmaf(a1, a1, ss); ss = __builtin_fmaf(a2, a2, ss); ss = __builtin_fmaf(a3, a3, ss);
    }
    s = xadd32(xadd16(s)); ss = xadd32(xadd16(ss));
    if (q == 0) stsc1_8(rsW, ST_OFF + (u32)((ct * S + T0 + 64 * th + tl) * 8), v2u{asu(s), asu(ss)});
  }
  WAITVN(0);
  WAITL0();
  publish(fl + 256, m, tag);

  {
    const int wa = (8 * hh + qp) * 192 + (q & 1) * 32 + pp * 8;
    bf16x8 wA[6], wB[6];
    auto w3_read1 = [&](bf16x8& dst, int base, int i) { const int o = base + (i & 1) * 12288 + (i >> 1) * 64; dst = tr2(o, 768); };
    poll(fl + 256, m, 16, tag);
    BAR();
    const int t32 = g * 4 + w;
    v2u st[4];
#pragma unroll
    for (int i = 0; i < 4; ++i) ldsc1_8(st[i], rsW, (u32)(((hh * 4 + i) * S + l32) * 8), ST_OFF + (u32)(t32 * 32) * 8);
    v4u hf[16];
    const u32 hb = H_OFF + (u32)(t32 * 32) * 1024;
    auto ldh = [&](auto i) { ldsc1<(i.v & 3) * 1024>(hf[i.v & 15], rsW, lane * 16, hb + (u32)(((i.v >> 2) + m) & 7) * 4096); };
    sfor<0, 16>(ldh);
#pragma unroll
    for (int i = 0; i < 6; ++i) w3_read1(wA[i], wa, i);
    asm volatile("s_waitcnt vmcnt(16)" : "+v"(st[0]), "+v"(st[1]), "+v"(st[2]), "+v"(st[3])::"memory");
    float rs, nb;
    {
      float s = (asf(st[0].x) + asf(st[1].x)) + (asf(st[2].x) + asf(st[3].x)), ss = (asf(st[0].y) + asf(st[1].y)) + (asf(st[2].y) + asf(st[3].y));
      s = xadd32(s); ss = xadd32(ss);
      const float mu = s * (1.0f / D);
      rs = __builtin_amdgcn_rsqf(fmaxf(__builtin_fmaf(-mu, mu, ss * (1.0f / D)), 0.f) + 1e-5f);
      nb = -mu * rs;
    }
    auto ln = [&](v4u h, int d) {
      const f32x2 v = {bflo(h[d]), bfhi(h[d])};
      const f32x2 r = __builtin_elementwise_fma(v, f32x2{rs, rs}, f32x2{nb, nb});
      return pack2(r[0], r[1]);
    };
    v4u yA, yB;
    wait_for<15>(hf[0]);
#pragma unroll
    for (int d = 0; d < 4; ++d) yA[d] = ln(hf[0], d);
    ldh(IC<16>{});
    f32x16 acc[6] = {};
    auto step = [&](auto sc, bf16x8(&wc)[6], bf16x8(&wn)[6], v4u& yc, v4u& yn) {
      constexpr int s = sc.v, c1 = (s + 1) >> 2;
      if constexpr (s + 1 < 32) wait_for<(16 + s < 31 ? 16 + s : 31) - (s + 1)>(hf[(s + 1) & 15]);
      if constexpr (s >= 4 && (s & 3) == 0) BAR();
      const int base = opq(wa + (c1 % 6) * SLOT + (16 * ((s + 1) & 3)) * 192);
      const bf16x8 yb = __builtin_bit_cast(bf16x8, yc);
      __builtin_amdgcn_s_waitcnt(0xC07F);
      sfor<0, 6>([&](auto ic) {
        constexpr int i = ic.v;
        acc[i] = mfma32(wc[i], yb, acc[i]);
        if constexpr (s + 1 < 32) {
          if constexpr (i < 3) { w3_read1(wn[2 * i], base, 2 * i); w3_read1(wn[2 * i + 1], base, 2 * i + 1); }
          if constexpr (i >= 2) yn[i - 2] = ln(hf[(s + 1) & 15], i - 2);
        }
        if constexpr (i == 5 && 17 + s < 32) ldh(IC<(17 + s) & 31>{});
        SCHED();
      });
    };
    sfor<0, 14>([&](auto k) { step(IC<2 * k.v>{}, wA, wB, yA, yB); step(IC<2 * k.v + 1>{}, wB, wA, yB, yA); });
    {
      BAR();
      v4u y4[4];
      y4[0] = yA;
      sfor<1, 4>([&](auto j) {
        wait_for<3 - j.v>(hf[(28 + j.v) & 15]);
#pragma unroll
        for (int d = 0; d < 4; ++d) y4[j.v][d] = ln(hf[(28 + j.v) & 15], d);
      });
      const int base = opq(wa + (7 % 6) * SLOT);
      auto epi = [&](int i, int s) {
        v4u o;
#pragma unroll
        for (int d = 0; d < 4; ++d) {
          const int r = 8 * s + 2 * d;
          o[d] = pack2(fmaxf(acc[2 * i][r], 0.f) * acc[2 * i + 1][r], fmaxf(acc[2 * i][r + 1], 0.f) * acc[2 * i + 1][r + 1]);
        }
        const int f0 = 96 * m + 32 * i;
        if (f0 < F) stsc1(rsW, A_OFF + (u32)((t32 * 86 + f0 / 16 + s) * 1024 + lane * 16), o);
      };
      bf16x8 wl[3][8];
      auto rd_pair = [&](int i) {
#pragma unroll
        for (int j = 0; j < 4; ++j) { w3_read1(wl[i][2 * j], base + 16 * j * 192, 2 * i); w3_read1(wl[i][2 * j + 1], base + 16 * j * 192, 2 * i + 1); }
      };
      rd_pair(0); rd_pair(1);
      SCHED();
      sfor<0, 3>([&](auto ic) {
        constexpr int i = ic.v;
        if constexpr (i == 0) rd_pair(2);
        sfor<0, 4>([&](auto jc) {
          constexpr int j = jc.v;
          const bf16x8 yb = __builtin_bit_cast(bf16x8, y4[j]);
          acc[2 * i] = mfma32(wl[i][2 * j], yb, acc[2 * i]);
          acc[2 * i + 1] = mfma32(wl[i][2 * j + 1], yb, acc[2 * i + 1]);
          if constexpr (i > 0 && j == 1) epi(i - 1, 0);
          if constexpr (i > 0 && j == 3) epi(i - 1, 1);
          SCHED();
        });
      });
      BAR();
      epi(2, 0); epi(2, 1);
    }
    WAITVN(0);
  }
  publish(fl + 512, m, tag);

  {
    poll(fl + 512, m, 15, tag);
    BAR();
    const int mh = w & 1, kh = w >> 1;
    const int t32 = g * 4 + 2 * th + mh;
    v4u af[36];
    const u32 ab = A_OFF + (u32)(t32 * 86 + kh * 43) * 1024;
    auto lda = [&](auto i) { const int ps = i.v + 5 * ct; ldsc1<0>(af[i.v % 36], rsW, lane * 16, ab + (u32)(ps >= 43 ? ps - 43 : ps) * 1024); };
    sfor<0, 14>(lda);
    f32x16 acc[2] = {};
    int wa[2];
#pragma unroll
    for (int n = 0; n < 2; ++n) wa[n] = (4 * hh + qp) * 128 + ((n ^ ((qp >> 1) & 1)) * 64) + (q & 1) * 32 + pp * 8;
    bf16x8 wA[2], wB[2];
    auto w4_read = [&](bf16x8(&dst)[2], auto sc) {
      constexpr int s = sc.v;
      const int off = s < 42 ? ((s / 6 + 2) % 6) * SLOT + kh * 12288 + (s % 6) * 2048 : M_W4T + kh * 2048;
#pragma unroll
      for (int n = 0; n < 2; ++n) dst[n] = tr2(opq(wa[n] + off), 1024);
    };
    w4_read(wA, IC<0>{});
    auto step = [&](auto sc, bf16x8(&wc)[2], bf16x8(&wn)[2]) {
      constexpr int s = sc.v;
      constexpr int issued = (14 + 2 * (s < 8 ? s : 8) + (s > 8 ? s - 8 : 0)) < 43 ? (14 + 2 * (s < 8 ? s : 8) + (s > 8 ? s - 8 : 0)) : 43;
      wait_for<issued - 1 - s>(af[s % 36]);
      if constexpr (s == 6 || s == 30) BAR();
      if constexpr (s + 1 < 43) w4_read(wn, IC<s + 1>{});
      const bf16x8 bfr = __builtin_bit_cast(bf16x8, af[s % 36]);
      acc[0] = mfma32(wc[0], bfr, acc[0]);
      if constexpr (issued < 43) lda(IC<(issued < 43 ? issued : 0)>{});
      SCHED();
      acc[1] = mfma32(wc[1], bfr, acc[1]);
      if constexpr (s < 8 && issued + 1 < 43) lda(IC<(issued + 1 < 43 ? issued + 1 : 0)>{});
      SCHED();
    };
    sfor<0, 21>([&](auto k) { step(IC<2 * k.v>{}, wA, wB); step(IC<2 * k.v + 1>{}, wB, wA); });
    step(IC<42>{}, wA, wB);
    LAS f32x4* red = lp<f32x4>(3 * SLOT);
    {
      const f32x16 snd = kh ? acc[0] : acc[1];
#pragma unroll
      for (int gg = 0; gg < 4; ++gg) red[(w * 4 + gg) * 64 + lane] = f32x4{snd[4 * gg], snd[4 * gg + 1], snd[4 * gg + 2], snd[4 * gg + 3]};
    }
    if (m == 0 && tid == 0) __hip_atomic_store(ep, tag, RLX_AGENT);
    WAITL0(); BAR();
    {
      const f32x16 keep = kh ? acc[1] : acc[0];
      const int pw = w ^ 2;
      const rsrc_t rsO = mkrs(p.out, S * D * 2);
      v2u r[4];
#pragma unroll
      for (int gg = 0; gg < 4; ++gg) {
        const f32x4 o = red[(pw * 4 + gg) * 64 + lane];
        const v2u hk = *lp<v2u>(M_HK + (32 * mh + l32) * HK_PITCH + (32 * kh + 8 * gg + 4 * hh) * 2);
        r[gg].x = pack2(bflo(hk.x) + (keep[4 * gg] + o[0]), bfhi(hk.x) + (keep[4 * gg + 1] + o[1]));
        r[gg].y = pack2(bflo(hk.y) + (keep[4 * gg + 2] + o[2]), bfhi(hk.y) + (keep[4 * gg + 3] + o[3]));
      }
      const u32 ob = (u32)(T0 + 64 * th + 32 * mh + l32) * (D * 2) + (u32)(64 * ct + 32 * kh + 8 * hh) * 2;
#pragma unroll
      for (int gp = 0; gp < 2; ++gp) {
        const auto sx = __builtin_amdgcn_permlane32_swap(r[2 * gp].x, r[2 * gp + 1].x, false, false);
        const auto sy = __builtin_amdgcn_permlane32_swap(r[2 * gp].y, r[2 * gp + 1].y, false, false);
        __builtin_amdgcn_raw_buffer_store_b128(v4u{sx[0], sy[0], sx[1], sy[1]}, rsO, ob + gp * 32, 0, 0);
      }
    }
  }
}

extern "C" void kernel_launch(void* const* d_in, const int* in_sizes, int n_in, void* d_out, int out_size,
                              void* d_ws, size_t ws_size, hipStream_t stream) {
  Params p;
  p.x = (const u16*)d_in[0]; p.wqkv = (const u16*)d_in[1]; p.wo = (const u16*)d_in[2];
  p.wg = (const u16*)d_in[4]; p.wu = (const u16*)d_in[5]; p.wd = (const u16*)d_in[6];
  p.out = (u16*)d_out; p.ws = (unsigned char*)d_ws;
  (void)hipFuncSetAttribute((const void*)fused_block, hipFuncAttributeMaxDynamicSharedMemorySize, LDS_BYTES);
  fused_block<<<256, 512, LDS_BYTES, stream>>>(p);
}
```
